```python
import torch

def _softmax(x, axis=-1):
    return torch.softmax(x, dim=axis)
def _gelu(x):
    return torch.nn.functional.gelu(x, approximate="tanh")
def _silu(x):
    return torch.nn.functional.silu(x)
def _sigmoid(x):
    return torch.sigmoid(x)
def _layernorm(x, axis=-1, eps=1e-5):
    m = x.mean(dim=axis, keepdim=True)
    v = x.var(dim=axis, keepdim=True, unbiased=False)
    return (x - m) / torch.sqrt(v + eps)
def _scatter_add(tgt, idx, src, axis=0):
    return tgt.clone().index_add_(axis, idx, src)
def _segment_sum(src, seg, n):
    out = torch.zeros((n,) + src.shape[1:], dtype=src.dtype)
    return out.index_add_(0, seg, src)
def _pad(x, pw):
    flat = tuple(v for pair in reversed(pw) for v in pair)
    return torch.nn.functional.pad(x, flat)

kernel_name = 'standalone__mha_softmax__near_ridge'

def setup_inputs():

    _w = (lambda a: (lambda t: t.to(torch.bfloat16) if t.is_floating_point() else t)(torch.as_tensor(a)))
    B, H, Sq, Sk, D = 8, 32, 736, 512, 128
    return {
        "q": _w((lambda *s: torch.randn(*s, dtype=torch.bfloat16) * 0.8)(B, H, Sq, D)),
        "k": _w((lambda *s: torch.randn(*s, dtype=torch.bfloat16) * 0.8)(B, H, Sk, D)),
        "v": _w((lambda *s: torch.randn(*s, dtype=torch.bfloat16) * 0.05)(B, H, Sk, D)),
    }

def reference(q, k, v):
    scale = q.shape[-1] ** -0.5
    scores = torch.einsum("bhqd,bhkd->bhqk", q, k) * scale
    p = _softmax(scores, axis=-1)
    return torch.einsum("bhqk,bhkd->bhqd", p, v)
```

```cpp
#include <hip/hip_runtime.h>
#include <hip/hip_bf16.h>
#include <cstdio>

using bf16 = __hip_bfloat16;
constexpr int   D = 128, NW = 8, QBLK = 32, KVBLK = 64;
constexpr float SCALE = 0.088388347648318440f;
#define ATTN_THR 8.f
constexpr float THR = ATTN_THR;
#define ATTN_LAYOUT 0
constexpr int LAYOUT = ATTN_LAYOUT;
#define ATTN_SDEPTH 2
constexpr int SDEPTH = ATTN_SDEPTH;
static_assert(SDEPTH == 1 || SDEPTH == 2, "SDEPTH is 1 or 2");
#define ATTN_B 8
#define ATTN_HQ 32
#define ATTN_HKV 32
#define ATTN_S 736
#define ATTN_SKV 512
constexpr int B = ATTN_B, HQ = ATTN_HQ, HKV = ATTN_HKV, S = ATTN_S, SKV = ATTN_SKV;
constexpr int LDQ = LAYOUT ? HQ * D : D, LDK = LAYOUT ? HKV * D : D, LDO = LDQ;

constexpr size_t SHM_V = KVBLK * D * 2, SHM_K = KVBLK * D * 2, SHM_ATTN = 2 * SHM_V + 2 * SHM_K + NW * 64 * 4;

using bf16x8 = __attribute__((ext_vector_type(8))) short;
using s16x4  = __attribute__((ext_vector_type(4))) short;
using f32x16 = __attribute__((ext_vector_type(16))) float;
using f32x8  = __attribute__((ext_vector_type(8))) float;
using u32x4  = __attribute__((ext_vector_type(4))) unsigned;
#define KSWZ(row, colB) ((row) * 256 + ((colB) ^ (((row) & 7) << 4)))
#define SBAR() __builtin_amdgcn_sched_barrier(0)
__device__ __forceinline__ int crow(int r, int hi) { return (r & 3) + 8 * (r >> 2) + 4 * hi; }
__device__ __forceinline__ unsigned cvtpk(float lo, float hi) {
  unsigned r; asm volatile("v_cvt_pk_bf16_f32 %0, %1, %2" : "=v"(r) : "v"(lo), "v"(hi)); return r;
}
template <typename TIn> struct Stage;
template <> struct Stage<bf16>  { using T = bf16x8;
  __device__ static __forceinline__ T ld8(const bf16* p) { return *reinterpret_cast<const bf16x8*>(p); }
  __device__ static __forceinline__ bf16x8 tobf(T x) { return x; } };
template <> struct Stage<float> { using T = f32x8;
  __device__ static __forceinline__ T ld8(const float* p) { return *reinterpret_cast<const f32x8*>(p); }
  __device__ static __forceinline__ bf16x8 tobf(T x) {
    u32x4 w = {cvtpk(x[0], x[1]), cvtpk(x[2], x[3]), cvtpk(x[4], x[5]), cvtpk(x[6], x[7])}; return *reinterpret_cast<bf16x8*>(&w); } };

__device__ __forceinline__ void partialSM(f32x16& p0, f32x16& p1, float& m_reg, float& mn, float& alpha) {
  constexpr float C = SCALE * 1.4426950408889634f;
  float pmax = p0[0]; for (int r = 1; r < 16; ++r) pmax = fmaxf(pmax, p0[r]); for (int r = 0; r < 16; ++r) pmax = fmaxf(pmax, p1[r]);
  { auto rr = __builtin_amdgcn_permlane32_swap(__float_as_uint(pmax), __float_as_uint(pmax), false, false);
    pmax = fmaxf(__uint_as_float(rr[0]), __uint_as_float(rr[1])); }
  if (__builtin_expect(__all(pmax - m_reg <= THR / SCALE), 1)) { mn = m_reg; alpha = 1.f; }
  else { mn = fmaxf(m_reg, pmax); alpha = __builtin_amdgcn_exp2f((m_reg - mn) * C); m_reg = mn; }
  float mnC = -mn * C;
  for (int r = 0; r < 16; ++r) p0[r] = fmaf(p0[r], C, mnC); for (int r = 0; r < 16; ++r) p1[r] = fmaf(p1[r], C, mnC);
  for (int r = 0; r < 16; ++r) p0[r] = __builtin_amdgcn_exp2f(p0[r]);
}
__device__ __forceinline__ void finishSM(f32x16& p0, f32x16& p1, float alpha, float& l_reg, bf16x8& pa0, bf16x8& pa1, bf16x8& pa2, bf16x8& pa3) {
  for (int r = 0; r < 16; ++r) p1[r] = __builtin_amdgcn_exp2f(p1[r]);
  float ps = 0; for (int r = 0; r < 16; ++r) ps += p0[r]; for (int r = 0; r < 16; ++r) ps += p1[r];
  { auto rr = __builtin_amdgcn_permlane32_swap(__float_as_uint(ps), __float_as_uint(ps), false, false);
    ps = __uint_as_float(rr[0]) + __uint_as_float(rr[1]); }
  l_reg = l_reg * alpha + ps;
#define PK4(P, BASE, OUT) do { unsigned a0 = cvtpk(P[BASE + 0], P[BASE + 1]), a1 = cvtpk(P[BASE + 2], P[BASE + 3]);   \
    unsigned b0 = cvtpk(P[BASE + 4], P[BASE + 5]), b1 = cvtpk(P[BASE + 6], P[BASE + 7]);                              \
    auto r0 = __builtin_amdgcn_permlane32_swap(a0, b0, false, false); auto r1 = __builtin_amdgcn_permlane32_swap(a1, b1, false, false); \
    u32x4 w = {r0[0], r1[0], r0[1], r1[1]}; OUT = *reinterpret_cast<bf16x8*>(&w); } while (0)
  PK4(p0, 0, pa0); PK4(p0, 8, pa1); PK4(p1, 0, pa2); PK4(p1, 8, pa3);
#undef PK4
}
__device__ __forceinline__ void qkt(f32x16& p0, f32x16& p1, const bf16* Ks, const bf16x8* qr, int r32, int hi) {
  p0 = f32x16{}; p1 = f32x16{};
  for (int d0 = 0; d0 < 8; ++d0) { int cb = (d0 * 16 + hi * 8) * 2;
    bf16x8 b0 = *reinterpret_cast<const bf16x8*>((const char*)Ks + KSWZ(r32, cb));
    bf16x8 b1 = *reinterpret_cast<const bf16x8*>((const char*)Ks + KSWZ(32 + r32, cb));
    p0 = __builtin_amdgcn_mfma_f32_32x32x16_bf16(b0, qr[d0], p0, 0, 0, 0);
    p1 = __builtin_amdgcn_mfma_f32_32x32x16_bf16(b1, qr[d0], p1, 0, 0, 0); }
}
__device__ __forceinline__ int v_st(int k, int c) { const int kk = (k & ~0xC) | ((k & 4) << 1) | ((k & 8) >> 1); return ((kk >> 3) * 4 + (c >> 5)) * 512 + ((kk & 7) * 32 + (c & 31)) * 2; }
__device__ __forceinline__ int v_rd_base(int lane) { return ((lane & 3) << 3) | (((lane >> 2) & 3) << 6) | (((lane >> 4) & 1) << 5) | (((lane >> 5) & 1) << 8); }
constexpr int v_rd_off(int d0, int ks, int half) { return d0 * 512 + ks * 4096 + half * 2048; }
template <int OFF> __device__ __forceinline__ s16x4 tr_read(int vb) {
  s16x4 r; asm volatile("ds_read_b64_tr_b16 %0, %1 offset:%2" : "=&v"(r) : "v"(vb), "i"(OFF) : "memory"); return r;
}
template <int D0> __device__ __forceinline__ void pv_one(f32x16& od, int vb, bf16x8 pa0, bf16x8 pa1, bf16x8 pa2, bf16x8 pa3) {
  const s16x4 l0 = tr_read<v_rd_off(D0, 0, 0)>(vb), h0 = tr_read<v_rd_off(D0, 0, 1)>(vb), l1 = tr_read<v_rd_off(D0, 1, 0)>(vb), h1 = tr_read<v_rd_off(D0, 1, 1)>(vb);
  const s16x4 l2 = tr_read<v_rd_off(D0, 2, 0)>(vb), h2 = tr_read<v_rd_off(D0, 2, 1)>(vb), l3 = tr_read<v_rd_off(D0, 3, 0)>(vb), h3 = tr_read<v_rd_off(D0, 3, 1)>(vb);
  asm volatile("s_waitcnt lgkmcnt(0)" ::: "memory"); SBAR();
#define PK(L, H) (bf16x8){L[0], L[1], L[2], L[3], H[0], H[1], H[2], H[3]}
  od = __builtin_amdgcn_mfma_f32_32x32x16_bf16(pa0, PK(l0, h0), od, 0, 0, 0);
  od = __builtin_amdgcn_mfma_f32_32x32x16_bf16(pa1, PK(l1, h1), od, 0, 0, 0);
  od = __builtin_amdgcn_mfma_f32_32x32x16_bf16(pa2, PK(l2, h2), od, 0, 0, 0);
  od = __builtin_amdgcn_mfma_f32_32x32x16_bf16(pa3, PK(l3, h3), od, 0, 0, 0);
#undef PK
}
__device__ __forceinline__ void pv_d0(f32x16* o, int vb, bf16x8 pa0, bf16x8 pa1, bf16x8 pa2, bf16x8 pa3) {
  pv_one<0>(o[0], vb, pa0, pa1, pa2, pa3); pv_one<1>(o[1], vb, pa0, pa1, pa2, pa3); pv_one<2>(o[2], vb, pa0, pa1, pa2, pa3); pv_one<3>(o[3], vb, pa0, pa1, pa2, pa3);
}

template <typename TQ>
__device__ __forceinline__ void attn_dense_body(const TQ* __restrict__ Qb, const bf16* __restrict__ Kh, const bf16* __restrict__ Vh,
                                                unsigned short* __restrict__ Ob, int seq, char* lds, int nrows) {
  using St = Stage<bf16>; using SQ = Stage<TQ>;
  const int tid = threadIdx.x, wid = tid >> 6, lane = tid & 63, r32 = lane & 31, hi = lane >> 5;
  bf16* V_lds = (bf16*)lds; bf16* K_lds = (bf16*)(lds + 2 * SHM_V);
  float* ws = (float*)(lds + 2 * SHM_V + 2 * SHM_K) + wid * 64; float* li_l = ws; float* al_l = ws + 32;
  float m_reg = -1e30f, l_reg = 0; f32x16 o[4] = {}; bf16x8 qr[8];
  const int qrow = min(wid * QBLK + r32, nrows - 1); const TQ* Qw = Qb + (long)qrow * LDQ + hi * 8;
#pragma unroll
  for (int d0 = 0; d0 < 8; ++d0) qr[d0] = SQ::tobf(SQ::ld8(Qw + d0 * 16));
  const int sr = tid >> 4, sc = (tid & 15) * 8, vst0 = v_st(sr, sc), vst1 = v_st(32 + sr, sc);
  const int vb0 = (int)(uintptr_t)V_lds + v_rd_base(lane);
  struct { typename St::T vs0, vs1, ks0, ks1; } sr_[SDEPTH];
#define SLOAD(i, k0) do { sr_[i].vs0 = St::ld8(&Vh[(long)((k0) + sr) * LDK + sc]); sr_[i].vs1 = St::ld8(&Vh[(long)((k0) + 32 + sr) * LDK + sc]); \
    sr_[i].ks0 = St::ld8(&Kh[(long)((k0) + sr) * LDK + sc]); sr_[i].ks1 = St::ld8(&Kh[(long)((k0) + 32 + sr) * LDK + sc]); } while (0)
#define SWRITE(b, i) do { *(bf16x8*)((char*)V_lds + (b) * SHM_V + vst0) = St::tobf(sr_[i].vs0);          \
    *(bf16x8*)((char*)V_lds + (b) * SHM_V + vst1) = St::tobf(sr_[i].vs1); int kc = sc * 2;               \
    *(bf16x8*)((char*)K_lds + (b) * SHM_K + KSWZ(sr, kc)) = St::tobf(sr_[i].ks0);                       \
    *(bf16x8*)((char*)K_lds + (b) * SHM_K + KSWZ(32 + sr, kc)) = St::tobf(sr_[i].ks1); } while (0)
#define SWAIT() do { if constexpr (SDEPTH == 2) asm volatile("s_waitcnt vmcnt(4)" ::: "memory"); else asm volatile("s_waitcnt vmcnt(0)" ::: "memory"); } while (0)
#define RESC(a) do { if (__any((a) < 1.f)) { if (hi == 0) al_l[r32] = (a); asm volatile("s_waitcnt lgkmcnt(0)" ::: "memory"); \
    for (int d = 0; d < 4; ++d) for (int r = 0; r < 16; ++r) o[d][r] *= al_l[crow(r, hi)]; } } while (0)
  f32x16 pA0, pA1, pB0, pB1; float mnA, mnB, alA, alB; bf16x8 pa0, pa1, pa2, pa3; const int NT = seq / KVBLK;
  constexpr int SE = 0, SO = SDEPTH - 1;
  SLOAD(SE, 0); asm volatile("s_waitcnt vmcnt(0)" ::: "memory"); SWRITE(0, SE); __syncthreads();
  qkt(pA0, pA1, K_lds, qr, r32, hi); partialSM(pA0, pA1, m_reg, mnA, alA);
  SLOAD(SO, KVBLK); if constexpr (SDEPTH == 2) { if (2 < NT) SLOAD(SE, 2 * KVBLK); }
  SWAIT(); SWRITE(1, SO); __syncthreads();
  for (int j = 1; j + 1 < NT; j += 2) {
    SBAR(); qkt(pB0, pB1, (bf16*)((char*)K_lds + SHM_K), qr, r32, hi);
    finishSM(pA0, pA1, alA, l_reg, pa0, pa1, pa2, pa3); SBAR();
    SLOAD(SO, (j + SDEPTH) * KVBLK); SBAR();
    pv_d0(o, vb0, pa0, pa1, pa2, pa3); partialSM(pB0, pB1, m_reg, mnB, alB);
    __syncthreads(); SWAIT(); SWRITE(0, SE);
    RESC(alB); __syncthreads();
    SBAR(); qkt(pA0, pA1, K_lds, qr, r32, hi);
    finishSM(pB0, pB1, alB, l_reg, pa0, pa1, pa2, pa3); SBAR();
    if (SDEPTH == 1 || j + 3 < NT) SLOAD(SE, (j + 1 + SDEPTH) * KVBLK); SBAR();
    pv_d0(o, vb0 + (int)SHM_V, pa0, pa1, pa2, pa3); partialSM(pA0, pA1, m_reg, mnA, alA);
    __syncthreads(); SWAIT(); SWRITE(1, SO);
    RESC(alA); __syncthreads();
  }
  SBAR(); qkt(pB0, pB1, (bf16*)((char*)K_lds + SHM_K), qr, r32, hi);
  finishSM(pA0, pA1, alA, l_reg, pa0, pa1, pa2, pa3); SBAR();
  pv_d0(o, vb0, pa0, pa1, pa2, pa3); partialSM(pB0, pB1, m_reg, mnB, alB);
  __syncthreads(); RESC(alB);
  finishSM(pB0, pB1, alB, l_reg, pa0, pa1, pa2, pa3); SBAR();
  pv_d0(o, vb0 + (int)SHM_V, pa0, pa1, pa2, pa3);
  if (hi == 0) li_l[r32] = l_reg; asm volatile("s_waitcnt lgkmcnt(0)" ::: "memory");
  float rli[16];
#pragma unroll
  for (int r = 0; r < 16; ++r) rli[r] = __builtin_amdgcn_rcpf(li_l[crow(r, hi)]);
  unsigned short* Ow = Ob + (long)(wid * QBLK) * LDO;
  if (wid * QBLK < nrows) {
#pragma unroll
  for (int r = 0; r < 16; ++r) { int orow = crow(r, hi);
    for (int d0 = 0; d0 < 4; ++d0) Ow[(long)orow * LDO + d0 * 32 + r32] = (unsigned short)(cvtpk(o[d0][r] * rli[r], 0.f) & 0xffff); }
  }
#undef SLOAD
#undef SWRITE
#undef SWAIT
#undef RESC
}

__global__ __launch_bounds__(64 * NW, 1)
void attn_fwd_dense(const bf16* __restrict__ Q, const bf16* __restrict__ K, const bf16* __restrict__ V, unsigned short* __restrict__ O) {
  extern __shared__ __attribute__((aligned(16))) char shm[];
  const int bid = blockIdx.x, xcd = bid & 7, j = bid >> 3;
  const int head = xcd * 32 + j / 3, blk = j % 3;
  const long i0 = (long)blk * 256;
  const long q0 = ((long)head * S + i0) * D, k0 = (long)head * SKV * D;
  attn_dense_body<bf16>(Q + q0, K + k0, V + k0, O + q0, SKV, shm, min(256, S - (int)i0));
}
extern "C" void kernel_launch(void* const* d_in, const int* in_sizes, int n_in, void* d_out, int out_size, void* d_ws, size_t ws_size, hipStream_t stream) {
  hipFuncSetAttribute((const void*)attn_fwd_dense, hipFuncAttributeMaxDynamicSharedMemorySize, (int)SHM_ATTN);
  hipLaunchKernelGGL(attn_fwd_dense, dim3(768), dim3(64 * NW), SHM_ATTN, stream,
                     (const bf16*)d_in[0], (const bf16*)d_in[1], (const bf16*)d_in[2], (unsigned short*)d_out);
}
```

```cpp
#include <hip/hip_runtime.h>
#include <hip/hip_bf16.h>

constexpr int SQ = 736, SK = 512, DH = 128;

__device__ __forceinline__ float bf_lo(unsigned u) { return __uint_as_float(u << 16); }
__device__ __forceinline__ float bf_hi(unsigned u) { return __uint_as_float(u & 0xffff0000u); }
__device__ __forceinline__ unsigned pk_bf16(float lo, float hi) { unsigned r; asm("v_cvt_pk_bf16_f32 %0, %1, %2" : "=v"(r) : "v"(lo), "v"(hi)); return r; }

typedef unsigned u32x4 __attribute__((ext_vector_type(4)));
__device__ __forceinline__ float dot_row(const u32x4* __restrict__ qp, const u32x4* __restrict__ kp) {
  float s = 0.f;
#pragma unroll 1
  for (int i = 0; i < DH / 8; ++i) {
    const u32x4 a = qp[i], b = kp[i];
#pragma unroll
    for (int e = 0; e < 4; ++e) { s = fmaf(bf_lo(a[e]), bf_lo(b[e]), s); s = fmaf(bf_hi(a[e]), bf_hi(b[e]), s); }
  }
  return s;
}
__device__ __forceinline__ void slow_rows(const unsigned* __restrict__ q, const unsigned* __restrict__ k, const unsigned* __restrict__ v,
                                          unsigned* __restrict__ o, int head, int row) {
  if (row >= SQ) return;
  const float c = 0.12751743082f;
  const u32x4* qp = (const u32x4*)(q + ((size_t)head * SQ + row) * (DH / 2));
  const u32x4* kp = (const u32x4*)(k + (size_t)head * SK * (DH / 2));
  const u32x4* vp = (const u32x4*)(v + (size_t)head * SK * (DH / 2));
  u32x4* op = (u32x4*)(o + ((size_t)head * SQ + row) * (DH / 2));
  float m = -INFINITY;
#pragma unroll 1
  for (int j = 0; j < SK; ++j) m = fmaxf(m, dot_row(qp, kp + j * (DH / 8)));
#pragma unroll 1
  for (int h = 0; h < 4; ++h) {
    float acc[32], l = 0.f;
#pragma unroll
    for (int i = 0; i < 32; ++i) acc[i] = 0.f;
#pragma unroll 1
    for (int j = 0; j < SK; ++j) {
      const float p = __builtin_amdgcn_exp2f((dot_row(qp, kp + j * (DH / 8)) - m) * c);
      l += p;
#pragma unroll
      for (int i = 0; i < 4; ++i) {
        const u32x4 b = vp[j * (DH / 8) + 4 * h + i];
#pragma unroll
        for (int e = 0; e < 4; ++e) { acc[8 * i + 2 * e] = fmaf(p, bf_lo(b[e]), acc[8 * i + 2 * e]); acc[8 * i + 2 * e + 1] = fmaf(p, bf_hi(b[e]), acc[8 * i + 2 * e + 1]); }
      }
    }
    const float il = 1.f / l;
#pragma unroll
    for (int i = 0; i < 4; ++i) {
      u32x4 r;
#pragma unroll
      for (int e = 0; e < 4; ++e) r[e] = pk_bf16(acc[8 * i + 2 * e] * il, acc[8 * i + 2 * e + 1] * il);
      op[4 * h + i] = r;
    }
  }
}

__global__ __launch_bounds__(256, 1) void mha_fwd(const unsigned* __restrict__ q, const unsigned* __restrict__ k, const unsigned* __restrict__ v,
                                                  unsigned* __restrict__ o) {
  const unsigned w = __builtin_amdgcn_readfirstlane(threadIdx.x >> 6);
  unsigned fm;
  asm volatile(
    "s_load_dwordx8 s[24:31], %[ka], 0\n"
    "s_lshr_b32 s11, %[blk], 3\n"
    "s_and_b32 s14, %[blk], 7\n"
    "s_lshl_b32 s14, s14, 5\n"
    "s_add_u32 s12, s11, 32\n"
    "s_add_u32 s13, s11, 64\n"
    "s_mul_i32 s8, s11, 171\n"
    "s_lshr_b32 s8, s8, 9\n"
    "s_mul_i32 s15, s8, 3\n"
    "s_sub_u32 s11, s11, s15\n"
    "s_add_u32 s8, s8, s14\n"
    "s_mul_i32 s9, s12, 171\n"
    "s_lshr_b32 s9, s9, 9\n"
    "s_mul_i32 s15, s9, 3\n"
    "s_sub_u32 s12, s12, s15\n"
    "s_add_u32 s9, s9, s14\n"
    "s_mul_i32 s10, s13, 171\n"
    "s_lshr_b32 s10, s10, 9\n"
    "s_mul_i32 s15, s10, 3\n"
    "s_sub_u32 s13, s13, s15\n"
    "s_add_u32 s10, s10, s14\n"
    "s_mov_b32 s81, %[w]\n"
    "v_mbcnt_lo_u32_b32 v236, -1, 0\n"
    "v_mbcnt_hi_u32_b32 v236, -1, v236\n"
    "v_bfe_u32 v237, v236, 2, 2\n"
    "v_lshrrev_b32 v238, 4, v236\n"
    "v_and_b32 v239, 3, v236\n"
    "v_lshlrev_b32 v227, 8, v237\n"
    "v_lshl_add_u32 v227, v238, 6, v227\n"
    "v_lshl_add_u32 v227, v239, 4, v227\n"
    "v_xor_b32 v228, 48, v227\n"
    "s_mov_b32 s38, 4096\n"
    "s_mov_b32 s39, 0x20000\n"
    "s_mov_b32 s42, 4096\n"
    "s_mov_b32 s43, 0x20000\n"
    "s_lshl_b32 s71, s81, 12\n"
    "s_lshl_b32 s72, s8, 17\n"
    "s_add_u32 s52, s72, s71\n"
    "s_mov_b32 s53, s52\n"
    "s_add_u32 m0, s71, 32768\n"
    "s_mov_b32 s57, s71\n"
    "s_add_u32 s80, s71, 49152\n"
    "s_mul_i32 s15, s8, 188416\n"
    "s_mov_b32 s50, 188416\n"
    "s_mov_b32 s51, 0x20000\n"
    "s_lshl_b32 s62, s81, 14\n"
    "s_lshl_b32 s79, s11, 16\n"
    "s_add_u32 s79, s79, s62\n"
    "s_mov_b32 s74, s79\n"
    "s_add_u32 s62, s62, 81920\n"
    "s_waitcnt lgkmcnt(0)\n"
    "s_add_u32 s36, s26, s52\n"
    "s_addc_u32 s37, s27, 0\n"
    "buffer_load_dwordx4 v227, s[36:39], 0 offen nt lds\n"
    "buffer_load_dwordx4 v227, s[36:39], 0 offen offset:1024 nt lds\n"
    "buffer_load_dwordx4 v228, s[36:39], 0 offen offset:2048 nt lds\n"
    "buffer_load_dwordx4 v228, s[36:39], 0 offen offset:3072 nt lds\n"
    "s_add_u32 s48, s24, s15\n"
    "s_addc_u32 s49, s25, 0\n"
    "s_add_u32 m0, s62, 0\n"
    "s_nop 0\n"
    "buffer_load_dwordx4 v227, s[48:51], s74 offen nt lds\n"
    "buffer_load_dwordx4 v227, s[48:51], s74 offen offset:1024 nt lds\n"
    "buffer_load_dwordx4 v228, s[48:51], s74 offen offset:2048 nt lds\n"
    "buffer_load_dwordx4 v228, s[48:51], s74 offen offset:3072 nt lds\n"
    "s_add_u32 m0, s62, 4096\n"
    "s_add_u32 s74, s74, 4096\n"
    "buffer_load_dwordx4 v227, s[48:51], s74 offen nt lds\n"
    "buffer_load_dwordx4 v227, s[48:51], s74 offen offset:1024 nt lds\n"
    "buffer_load_dwordx4 v228, s[48:51], s74 offen offset:2048 nt lds\n"
    "buffer_load_dwordx4 v228, s[48:51], s74 offen offset:3072 nt lds\n"
    "v_bfe_u32 v237, v236, 1, 3\n"
    "v_and_b32 v239, 1, v236\n"
    "v_lshlrev_b32 v229, 8, v237\n"
    "v_lshl_add_u32 v229, v238, 5, v229\n"
    "v_lshl_add_u32 v229, v239, 4, v229\n"
    "s_add_u32 s52, s52, 16384\n"
    "s_add_u32 s36, s26, s52\n"
    "s_addc_u32 s37, s27, 0\n"
    "s_add_u32 s40, s28, s53\n"
    "s_addc_u32 s41, s29, 0\n"
    "s_getpc_b64 s[22:23]\n"
    "LPC%=:\n"
    "v_lshlrev_b32 v237, 7, v236\n"
    "s_lshl_b32 s72, s81, 13\n"
    "v_add_u32 v237, s72, v237\n"
    "v_min_u32 v237, LEND%=-LPC%=, v237\n"
    "global_load_dword a243, v237, s[22:23]\n"
    "s_mov_b32 s54, 1610612736\n"
    "s_mov_b32 s55, 536870912\n"
    "s_add_u32 s92, s57, 49152\n"
    "s_mov_b32 s58, 0\n"
    "s_mov_b32 s59, 4294934528\n"
    "s_mov_b32 s60, s59\n"
    "s_lshl_b32 s56, s9, 17\n"
    "s_add_u32 s56, s56, s71\n"
    "s_lshl_b32 s78, s10, 17\n"
    "s_add_u32 s78, s78, s71\n"
    "s_mul_i32 s72, s8, 188416\n"
    "s_add_u32 s76, s30, s72\n"
    "s_addc_u32 s77, s31, 0\n"
    "s_mov_b32 s47, 0x20000\n"
    "s_mov_b32 s75, 0\n"
    "s_lshl_b32 s71, s81, 14\n"
    "s_mul_i32 s72, s9, 188416\n"
    "s_add_u32 s86, s30, s72\n"
    "s_addc_u32 s87, s31, 0\n"
    "s_lshl_b32 s88, s12, 16\n"
    "s_add_u32 s88, s88, s71\n"
    "s_add_u32 s72, s72, s88\n"
    "s_add_u32 s44, s24, s72\n"
    "s_addc_u32 s45, s25, 0\n"
    "s_sub_u32 s46, 188416, s88\n"
    "s_mul_i32 s72, s10, 188416\n"
    "s_add_u32 s89, s30, s72\n"
    "s_addc_u32 s90, s31, 0\n"
    "s_lshl_b32 s91, s13, 16\n"
    "s_add_u32 s91, s91, s71\n"
    "s_add_u32 s72, s72, s91\n"
    "s_add_u32 s82, s24, s72\n"
    "s_addc_u32 s83, s25, 0\n"
    "s_sub_u32 s84, 188416, s91\n"
    "s_add_u32 s72, s78, 98304\n"
    "s_add_u32 s18, s28, s72\n"
    "s_addc_u32 s19, s29, 0\n"
    "s_mov_b32 s20, 2048\n"
    "s_mov_b32 s17, 0\n"
    "s_cmp_eq_u32 s79, 180224\n"
    "s_cselect_b32 s61, 0xff800000, 0\n"
    "s_cmp_eq_u32 s88, 180224\n"
    "s_cselect_b32 s85, 0xff800000, 0\n"
    "s_cmp_eq_u32 s91, 180224\n"
    "s_cselect_b32 s99, 0xff800000, 0\n"
    "s_mov_b32 s73, s62\n"
    "s_mov_b32 s72, 16384\n"
    "s_mov_b32 s63, 0x17800000\n"
    "s_mov_b64 s[66:67], 0\n"
    "s_mov_b32 s68, 0\n"
    "s_mov_b32 s69, 0\n"
    "s_mov_b32 s9, s46\n"
    "s_mov_b32 s8, 133693440\n"
    "v_and_b32 v217, 15, v236\n"
    "v_lshrrev_b32 v237, 3, v217\n"
    "v_mul_u32_u24 v237, 3, v237\n"
    "v_xor_b32 v237, v237, v238\n"
    "v_lshlrev_b32 v224, 4, v237\n"
    "v_and_b32 v239, 3, v217\n"
    "v_lshl_add_u32 v224, v239, 6, v224\n"
    "v_lshrrev_b32 v237, 2, v217\n"
    "v_lshl_add_u32 v224, v237, 10, v224\n"
    "v_add_u32 v226, s62, v224\n"
    "v_add_u32 v224, 32768, v224\n"
    "v_lshlrev_b32 v225, 3, v239\n"
    "v_lshl_add_u32 v225, v237, 5, v225\n"
    "v_and_b32 v239, 1, v238\n"
    "v_lshl_add_u32 v225, v239, 7, v225\n"
    "v_lshrrev_b32 v237, 1, v238\n"
    "v_lshl_add_u32 v225, v237, 11, v225\n"
    "v_add_u32 v225, 49152, v225\n"
    "s_lshl_b32 s65, s81, 8\n"
    "s_add_u32 s65, s65, 147456\n"
    "v_lshl_add_u32 v240, v236, 2, s65\n"
    "v_and_b32 v241, 3, v236\n"
    "v_mul_u32_u24 v241, 260, v241\n"
    "v_add_u32 v241, 147456, v241\n"
    "v_mov_b32 v243, 0\n"
    "ds_write_b32 v240, v243\n"
    "v_and_b32 v219, 7, v217\n"
    "v_xor_b32 v219, v219, v237\n"
    "v_lshlrev_b32 v219, 4, v219\n"
    "v_lshlrev_b32 v221, 7, v217\n"
    "v_lshl_add_u32 v221, v239, 3, v221\n"
    "v_add_u32 v221, s62, v221\n"
    "v_xor_b32 v230, 0, v219\n"
    "v_add_u32 v230, v230, v221\n"
    "v_xor_b32 v231, 32, v219\n"
    "v_add_u32 v231, v231, v221\n"
    "v_xor_b32 v232, 64, v219\n"
    "v_add_u32 v232, v232, v221\n"
    "v_xor_b32 v233, 96, v219\n"
    "v_add_u32 v233, v233, v221\n"
    "v_lshlrev_b32 v234, 4, v236\n"
    "v_add_u32 v234, s62, v234\n"
    "v_add_u32 v234, 2048, v234\n"
    "v_lshrrev_b32 v237, 3, v236\n"
    "v_and_b32 v239, 7, v236\n"
    "v_xor_b32 v239, v239, v237\n"
    "v_lshlrev_b32 v235, 8, v237\n"
    "v_lshl_add_u32 v235, v239, 4, v235\n"
    "v_mov_b32 v237, 0x3f803f80\n"
    "v_accvgpr_write_b32 a176, v237\n"
    "v_accvgpr_write_b32 a177, v237\n"
    "v_accvgpr_write_b32 a178, v237\n"
    "v_accvgpr_write_b32 a179, v237\n"
    "v_lshrrev_b32 v219, 2, v217\n"
    "v_cmp_eq_u32 vcc, v219, v238\n"
    "v_and_b32 v219, 1, v217\n"
    "v_lshlrev_b32 v219, 4, v219\n"
    "v_bfe_u32 v239, v217, 1, 1\n"
    "v_cndmask_b32 v239, 2, v239, vcc\n"
    "v_mov_b32 v221, 15875\n"
    "v_lshlrev_b32 v221, v219, v221\n"
    "v_cmp_eq_u32 vcc, 0, v239\n"
    "s_nop 1\n"
    "v_cndmask_b32 v237, 0, v221, vcc\n"
    "v_accvgpr_write_b32 a246, v237\n"
    "v_cmp_eq_u32 vcc, 1, v239\n"
    "s_nop 1\n"
    "v_cndmask_b32 v237, 0, v221, vcc\n"
    "v_accvgpr_write_b32 a247, v237\n"
    "v_accvgpr_write_b32 a244, 0\n"
    "v_accvgpr_write_b32 a245, 0\n"
    "v_accvgpr_write_b32 a248, 0\n"
    "v_accvgpr_write_b32 a249, 0\n"
    "v_mov_b32 v221, 47576\n"
    "v_lshlrev_b32 v221, v219, v221\n"
    "v_cmp_eq_u32 vcc, 0, v239\n"
    "s_nop 1\n"
    "v_cndmask_b32 v237, 0, v221, vcc\n"
    "v_accvgpr_write_b32 a252, v237\n"
    "v_cmp_eq_u32 vcc, 1, v239\n"
    "s_nop 1\n"
    "v_cndmask_b32 v237, 0, v221, vcc\n"
    "v_accvgpr_write_b32 a253, v237\n"
    "v_accvgpr_write_b32 a250, 0\n"
    "v_accvgpr_write_b32 a251, 0\n"
    "v_accvgpr_write_b32 a254, 0\n"
    "v_accvgpr_write_b32 a255, 0\n"
    "v_lshrrev_b32 v219, 1, v238\n"
    "v_mul_u32_u24 v219, 3, v219\n"
    "v_bfe_u32 v221, v217, 1, 1\n"
    "v_xor_b32 v219, v219, v221\n"
    "v_lshlrev_b32 v219, 4, v219\n"
    "v_lshlrev_b32 v238, 10, v238\n"
    "v_lshrrev_b32 v221, 2, v217\n"
    "v_lshl_add_u32 v238, v221, 6, v238\n"
    "v_and_b32 v221, 1, v217\n"
    "v_lshl_add_u32 v238, v221, 3, v238\n"
    "v_add_u32 v238, 81920, v238\n"
    "v_xor_b32 v221, 32, v219\n"
    "v_add_u32 v239, v238, v221\n"
    "v_add_u32 v238, v238, v219\n"
    "v_lshlrev_b32 v236, 2, v217\n"
    "v_and_b32 v219, 1, v217\n"
    "v_mul_u32_u24 v219, 0xaaaa, v219\n"
    "v_add_u32 v219, 0x4444, v219\n"
    "v_lshl_add_u32 v237, v219, 16, v219\n"
    "v_accvgpr_write_b32 a160, 0\n"
    "v_accvgpr_write_b32 a161, 0\n"
    "v_accvgpr_write_b32 a162, 0\n"
    "v_accvgpr_write_b32 a163, 0\n"
    "v_accvgpr_write_b32 a164, 0\n"
    "v_accvgpr_write_b32 a165, 0\n"
    "v_accvgpr_write_b32 a166, 0\n"
    "v_accvgpr_write_b32 a167, 0\n"
    "v_accvgpr_write_b32 a168, 0\n"
    "v_accvgpr_write_b32 a169, 0\n"
    "v_accvgpr_write_b32 a170, 0\n"
    "v_accvgpr_write_b32 a171, 0\n"
    "v_accvgpr_write_b32 a172, 0\n"
    "v_accvgpr_write_b32 a173, 0\n"
    "v_accvgpr_write_b32 a174, 0\n"
    "v_accvgpr_write_b32 a175, 0\n"
    "s_waitcnt vmcnt(9)\n"
    "s_add_u32 m0, s62, 8192\n"
    "s_add_u32 s74, s74, 4096\n"
    "buffer_load_dwordx4 v227, s[48:51], s74 offen nt lds\n"
    "buffer_load_dwordx4 v227, s[48:51], s74 offen offset:1024 nt lds\n"
    "buffer_load_dwordx4 v228, s[48:51], s74 offen offset:2048 nt lds\n"
    "buffer_load_dwordx4 v228, s[48:51], s74 offen offset:3072 nt lds\n"
    "s_waitcnt lgkmcnt(0)\n"
    "s_barrier\n"
    "ds_read_b128 a[64:67], v224 offset:0\n"
    "ds_read_b128 a[68:71], v224 offset:256\n"
    "ds_read_b128 a[72:75], v224 offset:512\n"
    "ds_read_b128 a[76:79], v224 offset:768\n"
    "ds_read_b128 a[80:83], v224 offset:4096\n"
    "ds_read_b128 a[84:87], v224 offset:4352\n"
    "ds_read_b128 a[88:91], v224 offset:4608\n"
    "ds_read_b128 a[92:95], v224 offset:4864\n"
    "ds_read_b128 a[96:99], v224 offset:8192\n"
    "ds_read_b128 a[100:103], v224 offset:8448\n"
    "ds_read_b128 a[104:107], v224 offset:8704\n"
    "ds_read_b128 a[108:111], v224 offset:8960\n"
    "ds_read_b128 a[112:115], v224 offset:12288\n"
    "ds_read_b128 a[116:119], v224 offset:12544\n"
    "ds_read_b128 a[120:123], v224 offset:12800\n"
    "ds_read_b128 a[124:127], v224 offset:13056\n"
    "s_waitcnt vmcnt(9)\n"
    "ds_read_b128 a[0:3], v226 offset:0\n"
    "ds_read_b128 a[4:7], v226 offset:256\n"
    "ds_read_b128 a[8:11], v226 offset:512\n"
    "ds_read_b128 a[12:15], v226 offset:768\n"
    "s_add_u32 m0, s62, 12288\n"
    "s_add_u32 s74, s74, 4096\n"
    "buffer_load_dwordx4 v227, s[48:51], s74 offen nt lds\n"
    "buffer_load_dwordx4 v227, s[48:51], s74 offen offset:1024 nt lds\n"
    "buffer_load_dwordx4 v228, s[48:51], s74 offen offset:2048 nt lds\n"
    "buffer_load_dwordx4 v228, s[48:51], s74 offen offset:3072 nt lds\n"
    "s_waitcnt lgkmcnt(0)\n"
    "v_mfma_f32_16x16x32_bf16 v[16:19], a[246:249], a[0:3], 0\n"
    "v_mfma_f32_16x16x32_bf16 v[16:19], a[252:255], a[0:3], v[16:19]\n"
    "v_mfma_f32_16x16x32_bf16 v[20:23], a[244:247], a[0:3], 0\n"
    "v_mfma_f32_16x16x32_bf16 v[20:23], a[250:253], a[0:3], v[20:23]\n"
    "v_mfma_f32_16x16x32_bf16 v[24:27], a[246:249], a[4:7], 0\n"
    "v_mfma_f32_16x16x32_bf16 v[24:27], a[252:255], a[4:7], v[24:27]\n"
    "v_mfma_f32_16x16x32_bf16 v[28:31], a[244:247], a[4:7], 0\n"
    "v_mfma_f32_16x16x32_bf16 v[28:31], a[250:253], a[4:7], v[28:31]\n"
    "v_mfma_f32_16x16x32_bf16 v[32:35], a[246:249], a[8:11], 0\n"
    "s_nop 0\n"
    "v_cvt_pk_bf16_f32 v16, v16, v17\n"
    "v_accvgpr_write_b32 a0, v16\n"
    "v_mfma_f32_16x16x32_bf16 v[32:35], a[252:255], a[8:11], v[32:35]\n"
    "v_cvt_pk_bf16_f32 v17, v18, v19\n"
    "v_accvgpr_write_b32 a1, v17\n"
    "v_cvt_pk_bf16_f32 v24, v24, v25\n"
    "v_cvt_pk_bf16_f32 v25, v26, v27\n"
    "v_mfma_f32_16x16x32_bf16 v[36:39], a[244:247], a[8:11], 0\n"
    "v_cvt_pk_bf16_f32 v20, v20, v21\n"
    "v_cvt_pk_bf16_f32 v28, v28, v29\n"
    "v_mfma_f32_16x16x32_bf16 v[36:39], a[250:253], a[8:11], v[36:39]\n"
    "v_accvgpr_write_b32 a2, v20\n"
    "v_accvgpr_write_b32 a6, v28\n"
    "v_mfma_f32_16x16x32_bf16 v[40:43], a[246:249], a[12:15], 0\n"
    "v_cvt_pk_bf16_f32 v21, v22, v23\n"
    "v_accvgpr_write_b32 a3, v21\n"
    "v_cvt_pk_bf16_f32 v32, v32, v33\n"
    "v_mfma_f32_16x16x32_bf16 v[40:43], a[252:255], a[12:15], v[40:43]\n"
    "v_accvgpr_write_b32 a4, v24\n"
    "v_accvgpr_write_b32 a5, v25\n"
    "v_accvgpr_write_b32 a8, v32\n"
    "v_cvt_pk_bf16_f32 v36, v36, v37\n"
    "v_mfma_f32_16x16x32_bf16 v[44:47], a[244:247], a[12:15], 0\n"
    "v_cvt_pk_bf16_f32 v29, v30, v31\n"
    "v_accvgpr_write_b32 a7, v29\n"
    "v_cvt_pk_bf16_f32 v33, v34, v35\n"
    "v_accvgpr_write_b32 a9, v33\n"
    "v_mfma_f32_16x16x32_bf16 v[44:47], a[250:253], a[12:15], v[44:47]\n"
    "v_cvt_pk_bf16_f32 v37, v38, v39\n"
    "v_accvgpr_write_b32 a11, v37\n"
    "v_cvt_pk_bf16_f32 v40, v40, v41\n"
    "v_accvgpr_write_b32 a12, v40\n"
    "v_cvt_pk_bf16_f32 v41, v42, v43\n"
    "v_mfma_f32_16x16x32_bf16 v[0:3], a[64:67], a[0:3], 0\n"
    "v_accvgpr_write_b32 a10, v36\n"
    "v_accvgpr_write_b32 a13, v41\n"
    "v_mfma_f32_16x16x32_bf16 v[0:3], a[68:71], a[4:7], v[0:3]\n"
    "v_cvt_pk_bf16_f32 v44, v44, v45\n"
    "v_accvgpr_write_b32 a14, v44\n"
    "v_cvt_pk_bf16_f32 v45, v46, v47\n"
    "v_accvgpr_write_b32 a15, v45\n"
    "v_mfma_f32_16x16x32_bf16 v[0:3], a[72:75], a[8:11], v[0:3]\n"
    "s_nop 0\n"
    "v_mfma_f32_16x16x32_bf16 v[0:3], a[76:79], a[12:15], v[0:3]\n"
    "v_mfma_f32_16x16x32_bf16 v[4:7], a[80:83], a[0:3], 0\n"
    "v_mfma_f32_16x16x32_bf16 v[4:7], a[84:87], a[4:7], v[4:7]\n"
    "v_mfma_f32_16x16x32_bf16 v[4:7], a[88:91], a[8:11], v[4:7]\n"
    "s_nop 4\n"
    "v_max3_f32 v217, v0, v1, v2\n"
    "v_max_f32 v217, v217, v3\n"
    "v_mfma_f32_16x16x32_bf16 v[4:7], a[92:95], a[12:15], v[4:7]\n"
    "s_waitcnt vmcnt(9)\n"
    "ds_bpermute_b32 v217, v236, v217\n"
    "ds_read_b128 a[16:19], v226 offset:4096\n"
    "ds_read_b128 a[20:23], v226 offset:4352\n"
    "v_mfma_f32_16x16x32_bf16 v[8:11], a[96:99], a[0:3], 0\n"
    "ds_read_b128 a[24:27], v226 offset:4608\n"
    "ds_read_b128 a[28:31], v226 offset:4864\n"
    "v_mfma_f32_16x16x32_bf16 v[8:11], a[100:103], a[4:7], v[8:11]\n"
    "v_mfma_f32_16x16x32_bf16 v[8:11], a[104:107], a[8:11], v[8:11]\n"
    "v_mfma_f32_16x16x32_bf16 v[8:11], a[108:111], a[12:15], v[8:11]\n"
    "s_waitcnt lgkmcnt(4)\n"
    "v_sub_f32 v208, 0, v217\n"
    "v_add_f32 v6, v208, v6\n"
    "v_mfma_f32_16x16x32_bf16 v[12:15], a[112:115], a[0:3], 0\n"
    "v_add_f32 v0, v208, v0\n"
    "v_add_f32 v1, v208, v1\n"
    "v_mfma_f32_16x16x32_bf16 v[12:15], a[116:119], a[4:7], v[12:15]\n"
    "v_sub_f32 v209, 0, v217\n"
    "v_add_f32 v2, v208, v2\n"
    "v_mfma_f32_16x16x32_bf16 v[12:15], a[120:123], a[8:11], v[12:15]\n"
    "v_exp_f32 v1, v1\n"
    "v_mfma_f32_16x16x32_bf16 v[12:15], a[124:127], a[12:15], v[12:15]\n"
    "s_waitcnt lgkmcnt(3)\n"
    "v_exp_f32 v6, v6\n"
    "v_mfma_f32_16x16x32_bf16 v[16:19], a[246:249], a[16:19], 0\n"
    "v_add_f32 v4, v208, v4\n"
    "v_add_f32 v9, v208, v9\n"
    "v_mfma_f32_16x16x32_bf16 v[16:19], a[252:255], a[16:19], v[16:19]\n"
    "v_exp_f32 v0, v0\n"
    "v_mfma_f32_16x16x32_bf16 v[20:23], a[244:247], a[16:19], 0\n"
    "v_add_f32 v8, v208, v8\n"
    "v_add_f32 v12, v208, v12\n"
    "v_mfma_f32_16x16x32_bf16 v[20:23], a[250:253], a[16:19], v[20:23]\n"
    "s_waitcnt lgkmcnt(2)\n"
    "v_add_f32 v5, v208, v5\n"
    "v_add_f32 v10, v208, v10\n"
    "v_mfma_f32_16x16x32_bf16 v[24:27], a[246:249], a[20:23], 0\n"
    "s_mov_b32 m0, s57\n"
    "v_exp_f32 v9, v9\n"
    "v_add_f32 v11, v208, v11\n"
    "v_mfma_f32_16x16x32_bf16 v[24:27], a[252:255], a[20:23], v[24:27]\n"
    "v_cvt_pk_bf16_f32 v16, v16, v17\n"
    "v_cvt_pk_bf16_f32 v17, v18, v19\n"
    "v_exp_f32 v8, v8\n"
    "v_mfma_f32_16x16x32_bf16 v[28:31], a[244:247], a[20:23], 0\n"
    "v_accvgpr_write_b32 a16, v16\n"
    "v_cvt_pk_bf16_f32 v20, v20, v21\n"
    "v_mfma_f32_16x16x32_bf16 v[28:31], a[250:253], a[20:23], v[28:31]\n"
    "s_waitcnt lgkmcnt(1)\n"
    "v_exp_f32 v11, v11\n"
    "v_add_f32 v14, v208, v14\n"
    "v_mfma_f32_16x16x32_bf16 v[32:35], a[246:249], a[24:27], 0\n"
    "buffer_load_dwordx4 v227, s[36:39], 0 offen nt lds\n"
    "v_cvt_pk_bf16_f32 v24, v24, v25\n"
    "v_mfma_f32_16x16x32_bf16 v[32:35], a[252:255], a[24:27], v[32:35]\n"
    "v_accvgpr_write_b32 a17, v17\n"
    "v_accvgpr_write_b32 a18, v20\n"
    "v_exp_f32 v5, v5\n"
    "v_mfma_f32_16x16x32_bf16 v[36:39], a[244:247], a[24:27], 0\n"
    "buffer_load_dwordx4 v227, s[36:39], 0 offen offset:1024 nt lds\n"
    "v_cvt_pk_bf16_f32 v25, v26, v27\n"
    "v_exp_f32 v10, v10\n"
    "v_mfma_f32_16x16x32_bf16 v[36:39], a[250:253], a[24:27], v[36:39]\n"
    "s_waitcnt lgkmcnt(0)\n"
    "v_cvt_pk_bf16_f32 v21, v22, v23\n"
    "v_accvgpr_write_b32 a20, v24\n"
    "v_accvgpr_write_b32 a21, v25\n"
    "v_mfma_f32_16x16x32_bf16 v[40:43], a[246:249], a[28:31], 0\n"
    "buffer_load_dwordx4 v228, s[36:39], 0 offen offset:2048 nt lds\n"
    "v_cvt_pk_bf16_f32 v32, v32, v33\n"
    "v_accvgpr_write_b32 a24, v32\n"
    "v_mfma_f32_16x16x32_bf16 v[40:43], a[252:255], a[28:31], v[40:43]\n"
    "v_cvt_pk_bf16_f32 v28, v28, v29\n"
    "v_cvt_pk_bf16_f32 v33, v34, v35\n"
    "v_accvgpr_write_b32 a25, v33\n"
    "v_add_f32 v13, v208, v13\n"
    "v_mfma_f32_16x16x32_bf16 v[44:47], a[244:247], a[28:31], 0\n"
    "buffer_load_dwordx4 v228, s[36:39], 0 offen offset:3072 nt lds\n"
    "v_accvgpr_write_b32 a19, v21\n"
    "v_accvgpr_write_b32 a22, v28\n"
    "v_cvt_pk_bf16_f32 v36, v36, v37\n"
    "v_mfma_f32_16x16x32_bf16 v[44:47], a[250:253], a[28:31], v[44:47]\n"
    "s_add_u32 s52, s52, 16384\n"
    "v_cvt_pk_bf16_f32 v29, v30, v31\n"
    "v_accvgpr_write_b32 a23, v29\n"
    "v_accvgpr_write_b32 a26, v36\n"
    "v_cvt_pk_bf16_f32 v40, v40, v41\n"
    "v_accvgpr_write_b32 a28, v40\n"
    "v_cvt_pk_bf16_f32 v41, v42, v43\n"
    "v_accvgpr_write_b32 a29, v41\n"
    "v_exp_f32 v4, v4\n"
    "v_mfma_f32_16x16x32_bf16 v[192:195], a[64:67], a[16:19], 0\n"
    "s_add_u32 s57, s57, 16384\n"
    "v_cvt_pk_bf16_f32 v37, v38, v39\n"
    "v_accvgpr_write_b32 a27, v37\n"
    "v_mfma_f32_16x16x32_bf16 v[192:195], a[68:71], a[20:23], v[192:195]\n"
    "s_add_u32 s36, s26, s52\n"
    "v_cvt_pk_bf16_f32 v44, v44, v45\n"
    "v_accvgpr_write_b32 a30, v44\n"
    "v_cvt_pk_bf16_f32 v45, v46, v47\n"
    "v_accvgpr_write_b32 a31, v45\n"
    "v_mfma_f32_16x16x32_bf16 v[192:195], a[72:75], a[24:27], v[192:195]\n"
    "s_addc_u32 s37, s27, 0\n"
    "v_exp_f32 v14, v14\n"
    "v_mfma_f32_16x16x32_bf16 v[192:195], a[76:79], a[28:31], v[192:195]\n"
    "s_add_u32 m0, s80, 0\n"
    "v_exp_f32 v12, v12\n"
    "v_mfma_f32_16x16x32_bf16 v[196:199], a[80:83], a[16:19], 0\n"
    "buffer_load_dwordx4 v229, s[40:43], 0 offen nt lds\n"
    "v_add_f32 v7, v208, v7\n"
    "v_mfma_f32_16x16x32_bf16 v[196:199], a[84:87], a[20:23], v[196:199]\n"
    "v_exp_f32 v2, v2\n"
    "v_mfma_f32_16x16x32_bf16 v[196:199], a[88:91], a[24:27], v[196:199]\n"
    "buffer_load_dwordx4 v229, s[40:43], 0 offen offset:2048 nt lds\n"
    "v_max3_f32 v219, v192, v193, v194\n"
    "v_max_f32 v219, v219, v195\n"
    "v_mfma_f32_16x16x32_bf16 v[196:199], a[92:95], a[28:31], v[196:199]\n"
    "s_waitcnt vmcnt(10)\n"
    "ds_bpermute_b32 v219, v236, v219\n"
    "ds_read_b128 a[32:35], v226 offset:8192\n"
    "ds_read_b128 a[36:39], v226 offset:8448\n"
    "v_add_f32 v15, v208, v15\n"
    "v_mfma_f32_16x16x32_bf16 v[200:203], a[96:99], a[16:19], 0\n"
    "s_add_u32 m0, s80, 896\n"
    "ds_read_b128 a[40:43], v226 offset:8704\n"
    "ds_read_b128 a[44:47], v226 offset:8960\n"
    "v_add_f32 v3, v208, v3\n"
    "v_mfma_f32_16x16x32_bf16 v[200:203], a[100:103], a[20:23], v[200:203]\n"
    "buffer_load_dwordx4 v229, s[40:43], 0 offen offset:128 nt lds\n"
    "v_cvt_pk_bf16_f32 v128, v0, v1\n"
    "v_mfma_f32_16x16x32_bf16 v[200:203], a[104:107], a[24:27], v[200:203]\n"
    "v_exp_f32 v3, v3\n"
    "v_mfma_f32_16x16x32_bf16 v[200:203], a[108:111], a[28:31], v[200:203]\n"
    "buffer_load_dwordx4 v229, s[40:43], 0 offen offset:2176 nt lds\n"
    "v_cvt_pk_bf16_f32 v129, v2, v3\n"
    "v_mfma_f32_16x16x32_bf16 v[204:207], a[112:115], a[16:19], 0\n"
    "s_waitcnt lgkmcnt(4)\n"
    "v_sub_f32 v210, 0, v217\n"
    "v_sub_f32 v212, 0, v219\n"
    "v_mfma_f32_16x16x32_bf16 v[204:207], a[116:119], a[20:23], v[204:207]\n"
    "s_add_u32 s53, s53, 16384\n"
    "v_exp_f32 v7, v7\n"
    "v_mfma_f32_16x16x32_bf16 v[204:207], a[120:123], a[24:27], v[204:207]\n"
    "s_add_u32 s40, s28, s53\n"
    "v_exp_f32 v13, v13\n"
    "v_mfma_f32_16x16x32_bf16 v[204:207], a[124:127], a[28:31], v[204:207]\n"
    "s_waitcnt lgkmcnt(3)\n"
    "v_cvt_pk_bf16_f32 v132, v8, v9\n"
    "v_add_f32 v196, v212, v196\n"
    "v_mfma_f32_16x16x32_bf16 v[16:19], a[246:249], a[32:35], 0\n"
    "s_addc_u32 s41, s29, 0\n"
    "v_cvt_pk_bf16_f32 v133, v10, v11\n"
    "v_add_f32 v199, v212, v199\n"
    "v_mfma_f32_16x16x32_bf16 v[16:19], a[252:255], a[32:35], v[16:19]\n"
    "v_add_f32 v193, v212, v193\n"
    "v_add_f32 v198, v212, v198\n"
    "v_mfma_f32_16x16x32_bf16 v[20:23], a[244:247], a[32:35], 0\n"
    "v_add_f32 v195, v212, v195\n"
    "v_add_f32 v197, v212, v197\n"
    "v_add_f32 v204, v212, v204\n"
    "v_mfma_f32_16x16x32_bf16 v[20:23], a[250:253], a[32:35], v[20:23]\n"
    "s_waitcnt lgkmcnt(2)\n"
    "v_exp_f32 v196, v196\n"
    "v_add_f32 v200, v212, v200\n"
    "v_mfma_f32_16x16x32_bf16 v[24:27], a[246:249], a[36:39], 0\n"
    "v_cvt_pk_bf16_f32 v130, v4, v5\n"
    "v_exp_f32 v15, v15\n"
    "v_mfma_f32_16x16x32_bf16 v[24:27], a[252:255], a[36:39], v[24:27]\n"
    "v_exp_f32 v198, v198\n"
    "v_add_f32 v206, v212, v206\n"
    "v_mfma_f32_16x16x32_bf16 v[28:31], a[244:247], a[36:39], 0\n"
    "v_cvt_pk_bf16_f32 v20, v20, v21\n"
    "v_cvt_pk_bf16_f32 v135, v14, v15\n"
    "v_mfma_f32_16x16x32_bf16 v[28:31], a[250:253], a[36:39], v[28:31]\n"
    "s_waitcnt lgkmcnt(1)\n"
    "v_cvt_pk_bf16_f32 v16, v16, v17\n"
    "v_cvt_pk_bf16_f32 v134, v12, v13\n"
    "v_exp_f32 v206, v206\n"
    "v_add_f32 v207, v212, v207\n"
    "v_mfma_f32_16x16x32_bf16 v[32:35], a[246:249], a[40:43], 0\n"
    "v_exp_f32 v193, v193\n"
    "v_exp_f32 v200, v200\n"
    "v_mfma_f32_16x16x32_bf16 v[32:35], a[252:255], a[40:43], v[32:35]\n"
    "v_cvt_pk_bf16_f32 v21, v22, v23\n"
    "v_add_f32 v203, v212, v203\n"
    "v_mfma_f32_16x16x32_bf16 v[36:39], a[244:247], a[40:43], 0\n"
    "v_exp_f32 v199, v199\n"
    "v_mfma_f32_16x16x32_bf16 v[36:39], a[250:253], a[40:43], v[36:39]\n"
    "s_waitcnt lgkmcnt(0)\n"
    "v_cvt_pk_bf16_f32 v17, v18, v19\n"
    "v_cvt_pk_bf16_f32 v24, v24, v25\n"
    "v_cvt_pk_bf16_f32 v28, v28, v29\n"
    "v_sub_f32 v211, 0, v217\n"
    "v_cvt_pk_bf16_f32 v131, v6, v7\n"
    "v_add_f32 v192, v212, v192\n"
    "v_add_f32 v202, v212, v202\n"
    "v_mfma_f32_16x16x32_bf16 v[40:43], a[246:249], a[44:47], 0\n"
    "v_accvgpr_write_b32 a36, v24\n"
    "v_cvt_pk_bf16_f32 v25, v26, v27\n"
    "v_accvgpr_write_b32 a37, v25\n"
    "v_mfma_f32_16x16x32_bf16 v[40:43], a[252:255], a[44:47], v[40:43]\n"
    "v_accvgpr_write_b32 a32, v16\n"
    "v_accvgpr_write_b32 a34, v20\n"
    "v_cvt_pk_bf16_f32 v29, v30, v31\n"
    "v_accvgpr_write_b32 a39, v29\n"
    "v_mfma_f32_16x16x32_bf16 v[44:47], a[244:247], a[44:47], 0\n"
    "v_accvgpr_write_b32 a33, v17\n"
    "v_accvgpr_write_b32 a35, v21\n"
    "v_cvt_pk_bf16_f32 v36, v36, v37\n"
    "v_cvt_pk_bf16_f32 v37, v38, v39\n"
    "v_accvgpr_write_b32 a43, v37\n"
    "v_sub_f32 v215, 0, v219\n"
    "v_add_f32 v194, v212, v194\n"
    "v_mfma_f32_16x16x32_bf16 v[44:47], a[250:253], a[44:47], v[44:47]\n"
    "v_accvgpr_write_b32 a38, v28\n"
    "v_cvt_pk_bf16_f32 v32, v32, v33\n"
    "v_accvgpr_write_b32 a42, v36\n"
    "v_cvt_pk_bf16_f32 v40, v40, v41\n"
    "v_mfma_f32_16x16x32_bf16 v[0:3], a[64:67], a[32:35], 0\n"
    "v_accvgpr_write_b32 a40, v32\n"
    "v_cvt_pk_bf16_f32 v33, v34, v35\n"
    "v_accvgpr_write_b32 a41, v33\n"
    "v_mfma_f32_16x16x32_bf16 v[0:3], a[68:71], a[36:39], v[0:3]\n"
    "v_accvgpr_write_b32 a44, v40\n"
    "v_cvt_pk_bf16_f32 v41, v42, v43\n"
    "v_accvgpr_write_b32 a45, v41\n"
    "v_cvt_pk_bf16_f32 v44, v44, v45\n"
    "v_accvgpr_write_b32 a46, v44\n"
    "v_cvt_pk_bf16_f32 v45, v46, v47\n"
    "v_accvgpr_write_b32 a47, v45\n"
    "v_mfma_f32_16x16x32_bf16 v[0:3], a[72:75], a[40:43], v[0:3]\n"
    "v_exp_f32 v204, v204\n"
    "v_mfma_f32_16x16x32_bf16 v[0:3], a[76:79], a[44:47], v[0:3]\n"
    "v_exp_f32 v192, v192\n"
    "v_add_f32 v201, v212, v201\n"
    "v_mfma_f32_16x16x32_bf16 v[4:7], a[80:83], a[32:35], 0\n"
    "v_exp_f32 v197, v197\n"
    "v_mfma_f32_16x16x32_bf16 v[4:7], a[84:87], a[36:39], v[4:7]\n"
    "v_exp_f32 v194, v194\n"
    "v_exp_f32 v195, v195\n"
    "v_exp_f32 v207, v207\n"
    "v_mfma_f32_16x16x32_bf16 v[4:7], a[88:91], a[40:43], v[4:7]\n"
    "v_cvt_pk_bf16_f32 v136, v192, v193\n"
    "v_cvt_pk_bf16_f32 v137, v194, v195\n"
    "v_exp_f32 v202, v202\n"
    "v_max3_f32 v221, v0, v1, v2\n"
    "v_max_f32 v221, v221, v3\n"
    "v_mfma_f32_16x16x32_bf16 v[4:7], a[92:95], a[44:47], v[4:7]\n"
    "s_waitcnt vmcnt(8)\n"
    "ds_bpermute_b32 v221, v236, v221\n"
    "ds_read_b128 a[48:51], v226 offset:12288\n"
    "ds_read_b128 a[52:55], v226 offset:12544\n"
    "v_mfma_f32_16x16x32_bf16 v[8:11], a[96:99], a[32:35], 0\n"
    "ds_read_b128 a[56:59], v226 offset:12800\n"
    "ds_read_b128 a[60:63], v226 offset:13056\n"
    "v_exp_f32 v201, v201\n"
    "v_mfma_f32_16x16x32_bf16 v[8:11], a[100:103], a[36:39], v[8:11]\n"
    "v_sub_f32 v213, 0, v219\n"
    "v_cvt_pk_bf16_f32 v138, v196, v197\n"
    "v_mfma_f32_16x16x32_bf16 v[8:11], a[104:107], a[40:43], v[8:11]\n"
    "v_exp_f32 v203, v203\n"
    "v_mfma_f32_16x16x32_bf16 v[8:11], a[108:111], a[44:47], v[8:11]\n"
    "s_waitcnt lgkmcnt(4)\n"
    "v_sub_f32 v214, 0, v219\n"
    "v_sub_f32 v248, s61, v221\n"
    "v_add_f32 v7, v248, v7\n"
    "v_mfma_f32_16x16x32_bf16 v[12:15], a[112:115], a[32:35], 0\n"
    "v_cvt_pk_bf16_f32 v139, v198, v199\n"
    "v_cvt_pk_bf16_f32 v143, v206, v207\n"
    "v_mfma_f32_16x16x32_bf16 v[12:15], a[116:119], a[36:39], v[12:15]\n"
    "v_add_f32 v205, v212, v205\n"
    "v_add_f32 v1, v248, v1\n"
    "v_mfma_f32_16x16x32_bf16 v[12:15], a[120:123], a[40:43], v[12:15]\n"
    "v_add_f32 v0, v248, v0\n"
    "v_add_f32 v5, v248, v5\n"
    "v_mfma_f32_16x16x32_bf16 v[12:15], a[124:127], a[44:47], v[12:15]\n"
    "s_waitcnt lgkmcnt(3)\n"
    "v_sub_f32 v250, s61, v221\n"
    "v_exp_f32 v1, v1\n"
    "v_add_f32 v6, v248, v6\n"
    "v_mfma_f32_16x16x32_bf16 v[16:19], a[246:249], a[48:51], 0\n"
    "v_exp_f32 v0, v0\n"
    "v_mfma_f32_16x16x32_bf16 v[16:19], a[252:255], a[48:51], v[16:19]\n"
    "v_exp_f32 v5, v5\n"
    "v_mfma_f32_16x16x32_bf16 v[20:23], a[244:247], a[48:51], 0\n"
    "v_exp_f32 v205, v205\n"
    "v_add_f32 v9, v248, v9\n"
    "v_mfma_f32_16x16x32_bf16 v[20:23], a[250:253], a[48:51], v[20:23]\n"
    "s_waitcnt lgkmcnt(2)\n"
    "v_add_f32 v3, v248, v3\n"
    "v_exp_f32 v7, v7\n"
    "v_add_f32 v14, v248, v14\n"
    "v_mfma_f32_16x16x32_bf16 v[24:27], a[246:249], a[52:55], 0\n"
    "v_cvt_pk_bf16_f32 v16, v16, v17\n"
    "v_cvt_pk_bf16_f32 v141, v202, v203\n"
    "v_mfma_f32_16x16x32_bf16 v[24:27], a[252:255], a[52:55], v[24:27]\n"
    "v_add_f32 v8, v248, v8\n"
    "v_add_f32 v15, v248, v15\n"
    "v_mfma_f32_16x16x32_bf16 v[28:31], a[244:247], a[52:55], 0\n"
    "v_cvt_pk_bf16_f32 v17, v18, v19\n"
    "v_add_f32 v2, v248, v2\n"
    "v_add_f32 v12, v248, v12\n"
    "v_add_f32 v13, v248, v13\n"
    "v_mfma_f32_16x16x32_bf16 v[28:31], a[250:253], a[52:55], v[28:31]\n"
    "s_waitcnt lgkmcnt(1)\n"
    "v_cvt_pk_bf16_f32 v142, v204, v205\n"
    "v_exp_f32 v2, v2\n"
    "v_mfma_f32_16x16x32_bf16 v[32:35], a[246:249], a[56:59], 0\n"
    "v_exp_f32 v15, v15\n"
    "v_mfma_f32_16x16x32_bf16 v[32:35], a[252:255], a[56:59], v[32:35]\n"
    "v_cvt_pk_bf16_f32 v144, v0, v1\n"
    "v_add_f32 v11, v248, v11\n"
    "v_mfma_f32_16x16x32_bf16 v[36:39], a[244:247], a[56:59], 0\n"
    "v_cvt_pk_bf16_f32 v28, v28, v29\n"
    "v_add_f32 v4, v248, v4\n"
    "v_mfma_f32_16x16x32_bf16 v[36:39], a[250:253], a[56:59], v[36:39]\n"
    "s_waitcnt lgkmcnt(0)\n"
    "v_accvgpr_write_b32 a49, v17\n"
    "v_cvt_pk_bf16_f32 v24, v24, v25\n"
    "v_accvgpr_write_b32 a52, v24\n"
    "v_cvt_pk_bf16_f32 v29, v30, v31\n"
    "v_mfma_f32_16x16x32_bf16 v[40:43], a[246:249], a[60:63], 0\n"
    "s_mov_b32 m0, s57\n"
    "v_cvt_pk_bf16_f32 v20, v20, v21\n"
    "v_accvgpr_write_b32 a50, v20\n"
    "v_cvt_pk_bf16_f32 v21, v22, v23\n"
    "v_cvt_pk_bf16_f32 v25, v26, v27\n"
    "v_accvgpr_write_b32 a54, v28\n"
    "v_cvt_pk_bf16_f32 v32, v32, v33\n"
    "v_cvt_pk_bf16_f32 v33, v34, v35\n"
    "v_mfma_f32_16x16x32_bf16 v[40:43], a[252:255], a[60:63], v[40:43]\n"
    "v_accvgpr_write_b32 a48, v16\n"
    "v_cvt_pk_bf16_f32 v36, v36, v37\n"
    "v_sub_f32 v251, s61, v221\n"
    "v_mfma_f32_16x16x32_bf16 v[44:47], a[244:247], a[60:63], 0\n"
    "v_accvgpr_write_b32 a51, v21\n"
    "v_accvgpr_write_b32 a53, v25\n"
    "v_accvgpr_write_b32 a55, v29\n"
    "v_accvgpr_write_b32 a57, v33\n"
    "v_accvgpr_write_b32 a58, v36\n"
    "v_cvt_pk_bf16_f32 v37, v38, v39\n"
    "v_mfma_f32_16x16x32_bf16 v[44:47], a[250:253], a[60:63], v[44:47]\n"
    "v_cvt_pk_bf16_f32 v40, v40, v41\n"
    "v_accvgpr_write_b32 a60, v40\n"
    "v_cvt_pk_bf16_f32 v41, v42, v43\n"
    "v_mfma_f32_16x16x32_bf16 v[192:195], a[64:67], a[48:51], 0\n"
    "buffer_load_dwordx4 v227, s[36:39], 0 offen nt lds\n"
    "v_accvgpr_write_b32 a56, v32\n"
    "v_accvgpr_write_b32 a59, v37\n"
    "v_accvgpr_write_b32 a61, v41\n"
    "v_exp_f32 v3, v3\n"
    "v_mfma_f32_16x16x32_bf16 v[192:195], a[68:71], a[52:55], v[192:195]\n"
    "buffer_load_dwordx4 v227, s[36:39], 0 offen offset:1024 nt lds\n"
    "v_cvt_pk_bf16_f32 v44, v44, v45\n"
    "v_accvgpr_write_b32 a62, v44\n"
    "v_cvt_pk_bf16_f32 v45, v46, v47\n"
    "v_accvgpr_write_b32 a63, v45\n"
    "v_cvt_pk_bf16_f32 v140, v200, v201\n"
    "v_exp_f32 v6, v6\n"
    "v_mfma_f32_16x16x32_bf16 v[192:195], a[72:75], a[56:59], v[192:195]\n"
    "buffer_load_dwordx4 v228, s[36:39], 0 offen offset:2048 nt lds\n"
    "v_sub_f32 v249, s61, v221\n"
    "v_mfma_f32_16x16x32_bf16 v[192:195], a[76:79], a[60:63], v[192:195]\n"
    "buffer_load_dwordx4 v228, s[36:39], 0 offen offset:3072 nt lds\n"
    "v_add_f32 v10, v248, v10\n"
    "v_exp_f32 v10, v10\n"
    "v_exp_f32 v13, v13\n"
    "v_mfma_f32_16x16x32_bf16 v[196:199], a[80:83], a[48:51], 0\n"
    "s_add_u32 s52, s52, 16384\n"
    "v_cvt_pk_bf16_f32 v147, v6, v7\n"
    "v_exp_f32 v11, v11\n"
    "v_mfma_f32_16x16x32_bf16 v[196:199], a[84:87], a[52:55], v[196:199]\n"
    "s_add_u32 s57, s57, 16384\n"
    "v_exp_f32 v12, v12\n"
    "v_mfma_f32_16x16x32_bf16 v[196:199], a[88:91], a[56:59], v[196:199]\n"
    "s_add_u32 s36, s26, s52\n"
    "v_exp_f32 v4, v4\n"
    "v_max3_f32 v223, v192, v193, v194\n"
    "v_max_f32 v223, v223, v195\n"
    "v_mfma_f32_16x16x32_bf16 v[196:199], a[92:95], a[60:63], v[196:199]\n"
    "s_addc_u32 s37, s27, 0\n"
    "ds_bpermute_b32 v223, v236, v223\n"
    "v_mfma_f32_16x16x32_bf16 v[200:203], a[96:99], a[48:51], 0\n"
    "s_add_u32 m0, s80, 16384\n"
    "v_cvt_pk_bf16_f32 v145, v2, v3\n"
    "v_exp_f32 v8, v8\n"
    "v_cvt_pk_bf16_f32 v149, v10, v11\n"
    "v_exp_f32 v14, v14\n"
    "v_mfma_f32_16x16x32_bf16 v[200:203], a[100:103], a[52:55], v[200:203]\n"
    "buffer_load_dwordx4 v229, s[40:43], 0 offen nt lds\n"
    "v_exp_f32 v9, v9\n"
    "v_mfma_f32_16x16x32_bf16 v[200:203], a[104:107], a[56:59], v[200:203]\n"
    "buffer_load_dwordx4 v229, s[40:43], 0 offen offset:2048 nt lds\n"
    "v_cvt_pk_bf16_f32 v150, v12, v13\n"
    "v_mfma_f32_16x16x32_bf16 v[200:203], a[108:111], a[60:63], v[200:203]\n"
    "s_waitcnt lgkmcnt(0)\n"
    "s_add_u32 m0, s80, 17280\n"
    "v_cvt_pk_bf16_f32 v146, v4, v5\n"
    "v_sub_f32 v252, s61, v223\n"
    "v_add_f32 v194, v252, v194\n"
    "v_exp_f32 v194, v194\n"
    "v_add_f32 v198, v252, v198\n"
    "v_exp_f32 v198, v198\n"
    "v_mfma_f32_16x16x32_bf16 v[204:207], a[112:115], a[48:51], 0\n"
    "buffer_load_dwordx4 v229, s[40:43], 0 offen offset:128 nt lds\n"
    "v_cvt_pk_bf16_f32 v151, v14, v15\n"
    "v_sub_f32 v254, s61, v223\n"
    "v_add_f32 v195, v252, v195\n"
    "v_exp_f32 v195, v195\n"
    "v_add_f32 v196, v252, v196\n"
    "v_add_f32 v197, v252, v197\n"
    "v_exp_f32 v197, v197\n"
    "v_add_f32 v199, v252, v199\n"
    "v_mfma_f32_16x16x32_bf16 v[204:207], a[116:119], a[52:55], v[204:207]\n"
    "buffer_load_dwordx4 v229, s[40:43], 0 offen offset:2176 nt lds\n"
    "v_add_f32 v192, v252, v192\n"
    "v_add_f32 v201, v252, v201\n"
    "v_mfma_f32_16x16x32_bf16 v[204:207], a[120:123], a[56:59], v[204:207]\n"
    "s_mov_b32 s50, 0\n"
    "v_sub_f32 v255, s61, v223\n"
    "v_exp_f32 v192, v192\n"
    "v_add_f32 v193, v252, v193\n"
    "v_exp_f32 v193, v193\n"
    "v_cvt_pk_bf16_f32 v153, v194, v195\n"
    "v_exp_f32 v196, v196\n"
    "v_exp_f32 v199, v199\n"
    "v_add_f32 v200, v252, v200\n"
    "v_exp_f32 v200, v200\n"
    "v_exp_f32 v201, v201\n"
    "v_add_f32 v202, v252, v202\n"
    "v_exp_f32 v202, v202\n"
    "v_add_f32 v203, v252, v203\n"
    "v_exp_f32 v203, v203\n"
    "v_mfma_f32_16x16x32_bf16 v[204:207], a[124:127], a[60:63], v[204:207]\n"
    "v_cvt_pk_bf16_f32 v148, v8, v9\n"
    "v_sub_f32 v253, s61, v223\n"
    "v_cvt_pk_bf16_f32 v152, v192, v193\n"
    "v_cvt_pk_bf16_f32 v154, v196, v197\n"
    "v_cvt_pk_bf16_f32 v155, v198, v199\n"
    "v_cvt_pk_bf16_f32 v156, v200, v201\n"
    "v_cvt_pk_bf16_f32 v157, v202, v203\n"
    "s_waitcnt vmcnt(8)\n"
    "s_barrier\n"
    "v_add_u32 v224, s60, v224\n"
    "ds_read_b128 a[64:67], v224 offset:0\n"
    "ds_read_b128 a[68:71], v224 offset:256\n"
    "ds_read_b128 a[72:75], v224 offset:512\n"
    "ds_read_b128 a[76:79], v224 offset:768\n"
    "ds_read_b64_tr_b16 a[128:129], v225 offset:0\n"
    "ds_read_b64_tr_b16 a[130:131], v225 offset:4096\n"
    "ds_read_b64_tr_b16 a[132:133], v225 offset:8192\n"
    "ds_read_b64_tr_b16 a[134:135], v225 offset:12288\n"
    "ds_read_b64_tr_b16 a[136:137], v225 offset:256\n"
    "ds_read_b64_tr_b16 a[138:139], v225 offset:4352\n"
    "ds_read_b64_tr_b16 a[140:141], v225 offset:8448\n"
    "ds_read_b64_tr_b16 a[142:143], v225 offset:12544\n"
    "v_add_f32 v204, v252, v204\n"
    "v_add_f32 v205, v252, v205\n"
    "v_add_f32 v206, v252, v206\n"
    "v_add_f32 v207, v252, v207\n"
    "s_waitcnt lgkmcnt(11)\n"
    ".p2align 6\n"
    "LT0%=:\n"
    "v_mfma_f32_16x16x32_bf16 v[192:195], a[64:67], a[0:3], v[208:211]\n"
    "s_waitcnt lgkmcnt(1)\n"
    "ds_read_b64_tr_b16 a[148:149], v225 offset:8704\n"
    "v_mul_f32 v119, v218, v119\n"
    "v_mfma_f32_16x16x32_bf16 v[192:195], a[68:71], a[4:7], v[192:195]\n"
    "s_waitcnt lgkmcnt(1)\n"
    "ds_read_b64_tr_b16 a[144:145], v225 offset:512\n"
    "ds_read_b128 a[180:183], v234\n"
    "v_mfma_f32_16x16x32_bf16 v[192:195], a[72:75], a[8:11], v[192:195]\n"
    "s_waitcnt lgkmcnt(11)\n"
    "v_exp_f32 v206, v206\n"
    "v_mfma_f32_16x16x32_bf16 v[192:195], a[76:79], a[12:15], v[192:195]\n"
    "s_waitcnt lgkmcnt(9)\n"
    "v_exp_f32 v207, v207\n"
    "v_mfma_f32_16x16x32_bf16 v[0:3], a[128:131], v[128:131], 0\n"
    "s_waitcnt lgkmcnt(7)\n"
    "v_exp_f32 v204, v204\n"
    "v_mfma_f32_16x16x32_bf16 v[0:3], a[132:135], v[132:135], v[0:3]\n"
    "s_waitcnt lgkmcnt(0)\n"
    "buffer_store_dwordx4 a[180:183], v235, s[48:51], s74 offen nt\n"
    "v_mfma_f32_16x16x32_bf16 v[4:7], a[128:131], v[136:139], 0\n"
    "s_add_u32 s58, s58, 16384\n"
    "v_exp_f32 v205, v205\n"
    "v_mfma_f32_16x16x32_bf16 v[4:7], a[132:135], v[140:143], v[4:7]\n"
    "s_cmp_eq_u32 s58, 49152\n"
    "ds_read_b64_tr_b16 a[156:157], v225 offset:8960\n"
    "v_cvt_pk_bf16_f32 v158, v204, v205\n"
    "v_mfma_f32_16x16x32_bf16 v[196:199], a[64:67], a[16:19], v[212:215]\n"
    "s_cselect_b32 s60, s59, s72\n"
    "ds_read_b128 a[88:91], v224 offset:4608\n"
    "ds_read_b128 a[184:187], v234 offset:1024\n"
    "v_mfma_f32_16x16x32_bf16 v[196:199], a[68:71], a[20:23], v[196:199]\n"
    "s_cselect_b32 s58, 0, s58\n"
    "v_cvt_pk_bf16_f32 v159, v206, v207\n"
    "v_add_u32 v234, 2048, v234\n"
    "v_mfma_f32_16x16x32_bf16 v[196:199], a[72:75], a[24:27], v[196:199]\n"
    "v_exp_f32 v192, v192\n"
    "v_mfma_f32_16x16x32_bf16 v[196:199], a[76:79], a[28:31], v[196:199]\n"
    "v_exp_f32 v193, v193\n"
    "v_mfma_f32_16x16x32_bf16 v[8:11], a[128:131], v[144:147], 0\n"
    "ds_read_b64_tr_b16 a[146:147], v225 offset:4608\n"
    "ds_read_b64_tr_b16 a[158:159], v225 offset:13056\n"
    "v_mfma_f32_16x16x32_bf16 v[8:11], a[132:135], v[148:151], v[8:11]\n"
    "s_waitcnt lgkmcnt(2)\n"
    "buffer_store_dwordx4 a[184:187], v235, s[48:51], s74 offen offset:2048 nt\n"
    "v_mfma_f32_16x16x32_bf16 v[12:15], a[128:131], v[152:155], 0\n"
    "s_add_u32 s74, s74, 4096\n"
    "ds_read_b128 a[80:83], v224 offset:4096\n"
    "v_cvt_pk_bf16_f32 v160, v192, v193\n"
    "v_mfma_f32_16x16x32_bf16 v[12:15], a[132:135], v[156:159], v[12:15]\n"
    "s_mov_b32 m0, s57\n"
    "v_exp_f32 v198, v198\n"
    "v_mfma_f32_16x16x32_bf16 v[200:203], a[64:67], a[32:35], v[248:251]\n"
    "s_add_u32 s75, s75, 0x40000000\n"
    "ds_read_b128 a[84:87], v224 offset:4352\n"
    "v_mul_f32 v116, v218, v116\n"
    "v_mfma_f32_16x16x32_bf16 v[200:203], a[68:71], a[36:39], v[200:203]\n"
    "s_cselect_b32 s71, 16256, 0\n"
    "v_exp_f32 v199, v199\n"
    "v_mfma_f32_16x16x32_bf16 v[200:203], a[72:75], a[40:43], v[200:203]\n"
    "s_sub_u32 s74, s74, s71\n"
    "v_exp_f32 v194, v194\n"
    "v_mfma_f32_16x16x32_bf16 v[200:203], a[76:79], a[44:47], v[200:203]\n"
    "buffer_load_dwordx4 v227, s[36:39], 0 offen nt lds\n"
    "v_cvt_pk_bf16_f32 v169, v198, v199\n"
    "v_mfma_f32_16x16x32_bf16 v[16:19], a[136:139], v[128:131], 0\n"
    "v_exp_f32 v197, v197\n"
    "v_mfma_f32_16x16x32_bf16 v[16:19], a[140:143], v[132:135], v[16:19]\n"
    "v_exp_f32 v195, v195\n"
    "v_mfma_f32_16x16x32_bf16 v[20:23], a[136:139], v[136:139], 0\n"
    "v_cvt_pk_bf16_f32 v161, v194, v195\n"
    "v_mfma_f32_16x16x32_bf16 v[20:23], a[140:143], v[140:143], v[20:23]\n"
    "ds_read_b64_tr_b16 a[130:131], v225 offset:5120\n"
    "v_mul_f32 v120, v220, v120\n"
    "v_smfmac_f32_16x16x64_bf16 a[160:163], a[176:179], v[128:135], v237\n"
    "v_mul_f32 v122, v220, v122\n"
    "v_mfma_f32_16x16x32_bf16 v[204:207], a[64:67], a[48:51], v[252:255]\n"
    "ds_read_b64_tr_b16 a[152:153], v225 offset:768\n"
    "v_mfma_f32_16x16x32_bf16 v[204:207], a[68:71], a[52:55], v[204:207]\n"
    "ds_read_b128 a[92:95], v224 offset:4864\n"
    "buffer_load_dwordx4 v227, s[36:39], 0 offen offset:1024 nt lds\n"
    "v_mfma_f32_16x16x32_bf16 v[204:207], a[72:75], a[56:59], v[204:207]\n"
    "v_exp_f32 v201, v201\n"
    "v_mfma_f32_16x16x32_bf16 v[204:207], a[76:79], a[60:63], v[204:207]\n"
    "ds_read_b64_tr_b16 a[150:151], v225 offset:12800\n"
    "ds_read_b64_tr_b16 a[154:155], v225 offset:4864\n"
    "v_mfma_f32_16x16x32_bf16 v[24:27], a[136:139], v[144:147], 0\n"
    "v_exp_f32 v203, v203\n"
    "v_mfma_f32_16x16x32_bf16 v[24:27], a[140:143], v[148:151], v[24:27]\n"
    "v_mul_f32 v127, v222, v127\n"
    "v_mfma_f32_16x16x32_bf16 v[28:31], a[136:139], v[152:155], 0\n"
    "v_exp_f32 v196, v196\n"
    "v_mfma_f32_16x16x32_bf16 v[28:31], a[140:143], v[156:159], v[28:31]\n"
    "s_waitcnt lgkmcnt(6)\n"
    "v_exp_f32 v200, v200\n"
    "v_mfma_f32_16x16x32_bf16 v[192:195], a[80:83], a[0:3], v[208:211]\n"
    "s_waitcnt lgkmcnt(5)\n"
    "buffer_load_dwordx4 v228, s[36:39], 0 offen offset:2048 nt lds\n"
    "v_mul_f32 v123, v220, v123\n"
    "v_mfma_f32_16x16x32_bf16 v[192:195], a[84:87], a[4:7], v[192:195]\n"
    "v_mfma_f32_16x16x32_bf16 v[192:195], a[88:91], a[8:11], v[192:195]\n"
    "s_waitcnt lgkmcnt(2)\n"
    "v_cvt_pk_bf16_f32 v168, v196, v197\n"
    "v_mul_f32 v121, v220, v121\n"
    "v_mfma_f32_16x16x32_bf16 v[192:195], a[92:95], a[12:15], v[192:195]\n"
    "ds_read_b64_tr_b16 a[140:141], v225 offset:9472\n"
    "v_cvt_pk_bf16_f32 v176, v200, v201\n"
    "v_mfma_f32_16x16x32_bf16 v[32:35], a[144:147], v[128:131], 0\n"
    "s_waitcnt lgkmcnt(2)\n"
    "v_exp_f32 v205, v205\n"
    "v_mfma_f32_16x16x32_bf16 v[32:35], a[148:151], v[132:135], v[32:35]\n"
    "v_exp_f32 v207, v207\n"
    "v_mfma_f32_16x16x32_bf16 v[36:39], a[144:147], v[136:139], 0\n"
    "v_exp_f32 v202, v202\n"
    "v_mfma_f32_16x16x32_bf16 v[36:39], a[148:151], v[140:143], v[36:39]\n"
    "ds_read_b64_tr_b16 a[134:135], v225 offset:13312\n"
    "buffer_load_dwordx4 v228, s[36:39], 0 offen offset:3072 nt lds\n"
    "v_mfma_f32_16x16x32_bf16 v[196:199], a[80:83], a[16:19], v[212:215]\n"
    "s_add_u32 s52, s52, 16384\n"
    "v_exp_f32 v192, v192\n"
    "v_mfma_f32_16x16x32_bf16 v[196:199], a[84:87], a[20:23], v[196:199]\n"
    "s_add_u32 s54, s54, 0x20000000\n"
    "ds_read_b128 a[64:67], v224 offset:8192\n"
    "ds_read_b64_tr_b16 a[128:129], v225 offset:1024\n"
    "v_mfma_f32_16x16x32_bf16 v[196:199], a[88:91], a[24:27], v[196:199]\n"
    "s_cselect_b32 s52, s56, s52\n"
    "v_exp_f32 v193, v193\n"
    "v_mfma_f32_16x16x32_bf16 v[196:199], a[92:95], a[28:31], v[196:199]\n"
    "s_add_u32 s36, s26, s52\n"
    "v_cvt_pk_bf16_f32 v177, v202, v203\n"
    "v_mul_f32 v124, v222, v124\n"
    "v_mfma_f32_16x16x32_bf16 v[40:43], a[144:147], v[144:147], 0\n"
    "s_addc_u32 s37, s27, 0\n"
    "v_exp_f32 v204, v204\n"
    "v_mfma_f32_16x16x32_bf16 v[40:43], a[148:151], v[148:151], v[40:43]\n"
    "s_cmp_lt_u32 s52, 268435456\n"
    "ds_read_b128 a[68:71], v224 offset:8448\n"
    "ds_read_b128 a[76:79], v224 offset:8960\n"
    "v_mfma_f32_16x16x32_bf16 v[44:47], a[144:147], v[152:155], 0\n"
    "s_cselect_b32 s38, 4096, 0\n"
    "ds_read_b128 a[72:75], v224 offset:8704\n"
    "v_mul_f32 v118, v218, v118\n"
    "v_mfma_f32_16x16x32_bf16 v[44:47], a[148:151], v[156:159], v[44:47]\n"
    "s_add_u32 s57, s57, 16384\n"
    "v_exp_f32 v206, v206\n"
    "v_mfma_f32_16x16x32_bf16 v[200:203], a[80:83], a[32:35], v[248:251]\n"
    "s_cmp_ge_u32 s57, s92\n"
    "v_cvt_pk_bf16_f32 v185, v206, v207\n"
    "v_mul_f32 v117, v218, v117\n"
    "v_mfma_f32_16x16x32_bf16 v[200:203], a[84:87], a[36:39], v[200:203]\n"
    "s_cselect_b32 s71, 49152, 0\n"
    "ds_read_b64_tr_b16 a[148:149], v225 offset:9728\n"
    "v_cvt_pk_bf16_f32 v162, v192, v193\n"
    "v_mfma_f32_16x16x32_bf16 v[200:203], a[88:91], a[40:43], v[200:203]\n"
    "s_sub_u32 s57, s57, s71\n"
    "v_exp_f32 v197, v197\n"
    "v_mfma_f32_16x16x32_bf16 v[200:203], a[92:95], a[44:47], v[200:203]\n"
    "s_waitcnt lgkmcnt(8)\n"
    "v_exp_f32 v196, v196\n"
    "v_mfma_f32_16x16x32_bf16 v[48:51], a[152:155], v[128:131], 0\n"
    "s_add_u32 s8, s8, s8\n"
    "v_exp_f32 v198, v198\n"
    "v_mfma_f32_16x16x32_bf16 v[48:51], a[156:159], v[132:135], v[48:51]\n"
    "s_cselect_b32 s46, s9, 0\n"
    "ds_read_b64_tr_b16 a[132:133], v225 offset:9216\n"
    "v_add_u32 v243, 1, v243\n"
    "v_mfma_f32_16x16x32_bf16 v[52:55], a[152:155], v[136:139], 0\n"
    "s_cselect_b32 s10, 2048, 0\n"
    "ds_read_b64_tr_b16 a[136:137], v225 offset:1280\n"
    "ds_read_b64_tr_b16 a[144:145], v225 offset:1536\n"
    "v_mfma_f32_16x16x32_bf16 v[52:55], a[156:159], v[140:143], v[52:55]\n"
    "s_cselect_b32 s11, s20, 0\n"
    "ds_read_b64_tr_b16 a[138:139], v225 offset:5376\n"
    "v_cvt_pk_bf16_f32 v116, v116, v117\n"
    "v_smfmac_f32_16x16x64_bf16 a[164:167], a[176:179], v[136:143], v237\n"
    "s_cselect_b32 m0, s73, 148480\n"
    "v_cvt_pk_bf16_f32 v184, v204, v205\n"
    "v_mfma_f32_16x16x32_bf16 v[204:207], a[80:83], a[48:51], v[252:255]\n"
    "v_cvt_pk_bf16_f32 v170, v196, v197\n"
    "v_mfma_f32_16x16x32_bf16 v[204:207], a[84:87], a[52:55], v[204:207]\n"
    "v_exp_f32 v201, v201\n"
    "v_mfma_f32_16x16x32_bf16 v[204:207], a[88:91], a[56:59], v[204:207]\n"
    "buffer_load_dwordx4 v228, s[44:47], 0 offen nt lds\n"
    "v_mfma_f32_16x16x32_bf16 v[204:207], a[92:95], a[60:63], v[204:207]\n"
    "v_exp_f32 v195, v195\n"
    "v_mfma_f32_16x16x32_bf16 v[56:59], a[152:155], v[144:147], 0\n"
    "v_exp_f32 v194, v194\n"
    "v_mfma_f32_16x16x32_bf16 v[56:59], a[156:159], v[148:151], v[56:59]\n"
    "ds_read_b128 a[80:83], v224 offset:12288\n"
    "ds_read_b128 a[92:95], v224 offset:13056\n"
    "v_mfma_f32_16x16x32_bf16 v[60:63], a[152:155], v[152:155], 0\n"
    "v_exp_f32 v199, v199\n"
    "v_mfma_f32_16x16x32_bf16 v[60:63], a[156:159], v[156:159], v[60:63]\n"
    "s_waitcnt lgkmcnt(11)\n"
    "buffer_load_dwordx4 v228, s[44:47], 0 offen offset:1024 nt lds\n"
    "v_cvt_pk_bf16_f32 v163, v194, v195\n"
    "v_mfma_f32_16x16x32_bf16 v[192:195], a[64:67], a[0:3], v[208:211]\n"
    "s_waitcnt lgkmcnt(9)\n"
    "ds_read_b64_tr_b16 a[142:143], v225 offset:13568\n"
    "ds_read_b64_tr_b16 a[146:147], v225 offset:5632\n"
    "v_mfma_f32_16x16x32_bf16 v[192:195], a[68:71], a[4:7], v[192:195]\n"
    "s_waitcnt lgkmcnt(9)\n"
    "ds_read_b64_tr_b16 a[154:155], v225 offset:5888\n"
    "ds_read_b64_tr_b16 a[158:159], v225 offset:14080\n"
    "v_mfma_f32_16x16x32_bf16 v[192:195], a[72:75], a[8:11], v[192:195]\n"
    "s_add_u32 s73, s73, s10\n"
    "ds_read_b128 a[84:87], v224 offset:12544\n"
    "v_cvt_pk_bf16_f32 v117, v118, v119\n"
    "v_mfma_f32_16x16x32_bf16 v[192:195], a[76:79], a[12:15], v[192:195]\n"
    "s_add_u32 s44, s44, s11\n"
    "ds_read_b128 a[88:91], v224 offset:12800\n"
    "ds_read_b64_tr_b16 a[152:153], v225 offset:1792\n"
    "v_mfma_f32_16x16x32_bf16 v[64:67], a[128:131], v[128:131], 0\n"
    "s_waitcnt lgkmcnt(12)\n"
    "v_exp_f32 v205, v205\n"
    "v_mfma_f32_16x16x32_bf16 v[64:67], a[132:135], v[132:135], v[64:67]\n"
    "s_addc_u32 s45, s45, 0\n"
    "ds_read_b64_tr_b16 a[150:151], v225 offset:13824\n"
    "v_mov_b32 v223, v227\n"
    "v_mfma_f32_16x16x32_bf16 v[68:71], a[128:131], v[136:139], 0\n"
    "s_sub_i32 s9, s9, s10\n"
    "ds_read_b64_tr_b16 a[156:157], v225 offset:9984\n"
    "v_cvt_pk_bf16_f32 v171, v198, v199\n"
    "v_mfma_f32_16x16x32_bf16 v[68:71], a[132:135], v[140:143], v[68:71]\n"
    "s_waitcnt vmcnt(8)\n"
    "ds_write_b32 v240, v243\n"
    "v_mfma_f32_16x16x32_bf16 v[196:199], a[64:67], a[16:19], v[212:215]\n"
    "s_max_i32 s9, s9, 0\n"
    "v_exp_f32 v203, v203\n"
    "v_mfma_f32_16x16x32_bf16 v[196:199], a[68:71], a[20:23], v[196:199]\n"
    "s_add_u32 s53, s53, 16384\n"
    "v_exp_f32 v204, v204\n"
    "v_mfma_f32_16x16x32_bf16 v[196:199], a[72:75], a[24:27], v[196:199]\n"
    "s_add_u32 s55, s55, 0x20000000\n"
    "v_exp_f32 v200, v200\n"
    "v_mfma_f32_16x16x32_bf16 v[196:199], a[76:79], a[28:31], v[196:199]\n"
    "s_cselect_b32 s53, s56, s53\n"
    "v_cvt_pk_bf16_f32 v186, v204, v205\n"
    "v_mul_f32 v126, v222, v126\n"
    "v_mfma_f32_16x16x32_bf16 v[72:75], a[128:131], v[144:147], 0\n"
    "s_add_u32 s40, s28, s53\n"
    "v_exp_f32 v202, v202\n"
    "v_mfma_f32_16x16x32_bf16 v[72:75], a[132:135], v[148:151], v[72:75]\n"
    "s_addc_u32 s41, s29, 0\n"
    "v_cvt_pk_bf16_f32 v178, v200, v201\n"
    "v_cvt_pk_bf16_f32 v179, v202, v203\n"
    "v_mfma_f32_16x16x32_bf16 v[76:79], a[128:131], v[152:155], 0\n"
    "s_cmp_lt_u32 s53, 268435456\n"
    "v_exp_f32 v194, v194\n"
    "v_mfma_f32_16x16x32_bf16 v[76:79], a[132:135], v[156:159], v[76:79]\n"
    "s_cselect_b32 s42, 4096, 0\n"
    "v_exp_f32 v207, v207\n"
    "v_mfma_f32_16x16x32_bf16 v[200:203], a[64:67], a[32:35], v[248:251]\n"
    "v_exp_f32 v206, v206\n"
    "v_mfma_f32_16x16x32_bf16 v[200:203], a[68:71], a[36:39], v[200:203]\n"
    "v_exp_f32 v192, v192\n"
    "v_mfma_f32_16x16x32_bf16 v[200:203], a[72:75], a[40:43], v[200:203]\n"
    "v_exp_f32 v195, v195\n"
    "v_mfma_f32_16x16x32_bf16 v[200:203], a[76:79], a[44:47], v[200:203]\n"
    "s_waitcnt lgkmcnt(12)\n"
    "v_cvt_pk_bf16_f32 v187, v206, v207\n"
    "v_add_u32 v224, s60, v224\n"
    "v_mfma_f32_16x16x32_bf16 v[80:83], a[136:139], v[128:131], 0\n"
    "s_waitcnt lgkmcnt(9)\n"
    "v_exp_f32 v193, v193\n"
    "v_mfma_f32_16x16x32_bf16 v[80:83], a[140:143], v[132:135], v[80:83]\n"
    "v_exp_f32 v199, v199\n"
    "v_mfma_f32_16x16x32_bf16 v[84:87], a[136:139], v[136:139], 0\n"
    "v_exp_f32 v198, v198\n"
    "v_mfma_f32_16x16x32_bf16 v[84:87], a[140:143], v[140:143], v[84:87]\n"
    "ds_write_b64 v233, v[116:117] offset:10240\n"
    "v_smfmac_f32_16x16x64_bf16 a[168:171], a[176:179], v[144:151], v237\n"
    "v_mul_f32 v125, v222, v125\n"
    "v_mfma_f32_16x16x32_bf16 v[204:207], a[64:67], a[48:51], v[252:255]\n"
    "v_exp_f32 v200, v200\n"
    "v_mfma_f32_16x16x32_bf16 v[204:207], a[68:71], a[52:55], v[204:207]\n"
    "ds_read_b32 v242, v241\n"
    "v_cvt_pk_bf16_f32 v173, v198, v199\n"
    "v_mfma_f32_16x16x32_bf16 v[204:207], a[72:75], a[56:59], v[204:207]\n"
    "v_exp_f32 v203, v203\n"
    "v_mfma_f32_16x16x32_bf16 v[204:207], a[76:79], a[60:63], v[204:207]\n"
    "v_exp_f32 v202, v202\n"
    "v_mfma_f32_16x16x32_bf16 v[88:91], a[136:139], v[144:147], 0\n"
    "v_exp_f32 v197, v197\n"
    "v_mfma_f32_16x16x32_bf16 v[88:91], a[140:143], v[148:151], v[88:91]\n"
    "v_exp_f32 v196, v196\n"
    "v_mfma_f32_16x16x32_bf16 v[92:95], a[136:139], v[152:155], 0\n"
    "v_cvt_pk_bf16_f32 v165, v194, v195\n"
    "v_cvt_pk_bf16_f32 v120, v120, v121\n"
    "v_mfma_f32_16x16x32_bf16 v[92:95], a[140:143], v[156:159], v[92:95]\n"
    "s_waitcnt lgkmcnt(0)\n"
    "v_cvt_pk_bf16_f32 v164, v192, v193\n"
    "v_cmp_gt_u32 vcc, v243, v242\n"
    "v_mfma_f32_16x16x32_bf16 v[192:195], a[80:83], a[0:3], v[208:211]\n"
    "s_cbranch_vccnz LSPT0%=\n"
    "LSRT0%=:\n"
    "v_exp_f32 v204, v204\n"
    "v_mfma_f32_16x16x32_bf16 v[192:195], a[84:87], a[4:7], v[192:195]\n"
    "s_add_u32 m0, s80, 0\n"
    "v_cvt_pk_bf16_f32 v172, v196, v197\n"
    "v_cvt_pk_bf16_f32 v124, v124, v125\n"
    "v_mfma_f32_16x16x32_bf16 v[192:195], a[88:91], a[8:11], v[192:195]\n"
    "ds_read_b64_tr_b16 a[140:141], v225 offset:24832\n"
    "v_exp_f32 v205, v205\n"
    "v_mfma_f32_16x16x32_bf16 v[192:195], a[92:95], a[12:15], v[192:195]\n"
    "buffer_load_dwordx4 v229, s[40:43], 0 offen nt lds\n"
    "v_cvt_pk_bf16_f32 v181, v202, v203\n"
    "v_mfma_f32_16x16x32_bf16 v[96:99], a[144:147], v[128:131], 0\n"
    "ds_read_b64_tr_b16 a[134:135], v225 offset:28672\n"
    "v_cvt_pk_bf16_f32 v121, v122, v123\n"
    "v_mfma_f32_16x16x32_bf16 v[96:99], a[148:151], v[132:135], v[96:99]\n"
    "ds_read_b128 a[68:71], v224 offset:256\n"
    "ds_read_b64_tr_b16 a[130:131], v225 offset:20480\n"
    "v_cvt_pk_bf16_f32 v125, v126, v127\n"
    "v_mfma_f32_16x16x32_bf16 v[100:103], a[144:147], v[136:139], 0\n"
    "v_exp_f32 v201, v201\n"
    "v_mfma_f32_16x16x32_bf16 v[100:103], a[148:151], v[140:143], v[100:103]\n"
    "ds_read_b128 a[72:75], v224 offset:512\n"
    "ds_read_b64_tr_b16 a[136:137], v225 offset:16640\n"
    "v_mfma_f32_16x16x32_bf16 v[196:199], a[80:83], a[16:19], v[212:215]\n"
    "ds_read_b64_tr_b16 a[132:133], v225 offset:24576\n"
    "ds_read_b64_tr_b16 a[142:143], v225 offset:28928\n"
    "v_mfma_f32_16x16x32_bf16 v[196:199], a[84:87], a[20:23], v[196:199]\n"
    "v_exp_f32 v193, v193\n"
    "v_mfma_f32_16x16x32_bf16 v[196:199], a[88:91], a[24:27], v[196:199]\n"
    "ds_read_b128 a[76:79], v224 offset:768\n"
    "ds_read_b64_tr_b16 a[138:139], v225 offset:20736\n"
    "v_mfma_f32_16x16x32_bf16 v[196:199], a[92:95], a[28:31], v[196:199]\n"
    "buffer_load_dwordx4 v229, s[40:43], 0 offen offset:2048 nt lds\n"
    "v_exp_f32 v206, v206\n"
    "v_mfma_f32_16x16x32_bf16 v[104:107], a[144:147], v[144:147], 0\n"
    "s_add_u32 m0, s80, 896\n"
    "v_exp_f32 v195, v195\n"
    "v_mfma_f32_16x16x32_bf16 v[104:107], a[148:151], v[148:151], v[104:107]\n"
    "v_cvt_pk_bf16_f32 v180, v200, v201\n"
    "v_cvt_pk_bf16_f32 v188, v204, v205\n"
    "v_mfma_f32_16x16x32_bf16 v[108:111], a[144:147], v[152:155], 0\n"
    "ds_write_b64 v233, v[120:121] offset:12288\n"
    "v_mfma_f32_16x16x32_bf16 v[108:111], a[148:151], v[156:159], v[108:111]\n"
    "v_exp_f32 v207, v207\n"
    "v_mfma_f32_16x16x32_bf16 v[200:203], a[80:83], a[32:35], v[248:251]\n"
    "v_exp_f32 v194, v194\n"
    "v_mfma_f32_16x16x32_bf16 v[200:203], a[84:87], a[36:39], v[200:203]\n"
    "v_exp_f32 v198, v198\n"
    "v_mfma_f32_16x16x32_bf16 v[200:203], a[88:91], a[40:43], v[200:203]\n"
    "ds_write_b64 v233, v[124:125] offset:14336\n"
    "v_mfma_f32_16x16x32_bf16 v[200:203], a[92:95], a[44:47], v[200:203]\n"
    "buffer_load_dwordx4 v229, s[40:43], 0 offen offset:128 nt lds\n"
    "v_cvt_pk_bf16_f32 v167, v194, v195\n"
    "v_mfma_f32_16x16x32_bf16 v[112:115], a[152:155], v[128:131], 0\n"
    "v_exp_f32 v197, v197\n"
    "v_mfma_f32_16x16x32_bf16 v[112:115], a[156:159], v[132:135], v[112:115]\n"
    "ds_read_b128 a[64:67], v224 offset:0\n"
    "ds_read_b64_tr_b16 a[128:129], v225 offset:16384\n"
    "v_mfma_f32_16x16x32_bf16 v[116:119], a[152:155], v[136:139], 0\n"
    "v_exp_f32 v196, v196\n"
    "v_mfma_f32_16x16x32_bf16 v[116:119], a[156:159], v[140:143], v[116:119]\n"
    "v_exp_f32 v203, v203\n"
    "v_smfmac_f32_16x16x64_bf16 a[172:175], a[176:179], v[152:159], v237\n"
    "v_cvt_pk_bf16_f32 v189, v206, v207\n"
    "v_mfma_f32_16x16x32_bf16 v[204:207], a[80:83], a[48:51], v[252:255]\n"
    "v_exp_f32 v192, v192\n"
    "v_mfma_f32_16x16x32_bf16 v[204:207], a[84:87], a[52:55], v[204:207]\n"
    "v_exp_f32 v202, v202\n"
    "v_mfma_f32_16x16x32_bf16 v[204:207], a[88:91], a[56:59], v[204:207]\n"
    "buffer_load_dwordx4 v229, s[40:43], 0 offen offset:2176 nt lds\n"
    "v_cvt_pk_bf16_f32 v166, v192, v193\n"
    "v_mfma_f32_16x16x32_bf16 v[204:207], a[92:95], a[60:63], v[204:207]\n"
    "s_lshr_b32 s70, s69, 1\n"
    "v_exp_f32 v199, v199\n"
    "v_mfma_f32_16x16x32_bf16 v[120:123], a[152:155], v[144:147], 0\n"
    "v_cvt_pk_bf16_f32 v174, v196, v197\n"
    "v_cvt_pk_bf16_f32 v183, v202, v203\n"
    "v_mfma_f32_16x16x32_bf16 v[120:123], a[156:159], v[148:151], v[120:123]\n"
    "v_exp_f32 v200, v200\n"
    "v_mfma_f32_16x16x32_bf16 v[124:127], a[152:155], v[152:155], 0\n"
    "v_exp_f32 v201, v201\n"
    "v_mfma_f32_16x16x32_bf16 v[124:127], a[156:159], v[156:159], v[124:127]\n"
    "s_waitcnt lgkmcnt(1)\n"
    "v_cvt_pk_bf16_f32 v175, v198, v199\n"
    "v_cvt_pk_bf16_f32 v182, v200, v201\n"
    ".p2align 6\n"
    "LTO%=:\n"
    "v_mfma_f32_16x16x32_bf16 v[192:195], a[64:67], a[0:3], v[208:211]\n"
    "v_mfma_f32_16x16x32_bf16 v[192:195], a[68:71], a[4:7], v[192:195]\n"
    "s_add_u32 s58, s58, 16384\n"
    "ds_read_b128 a[180:183], v234\n"
    "v_mfma_f32_16x16x32_bf16 v[192:195], a[72:75], a[8:11], v[192:195]\n"
    "s_cmp_eq_u32 s58, 49152\n"
    "v_exp_f32 v205, v205\n"
    "v_mfma_f32_16x16x32_bf16 v[192:195], a[76:79], a[12:15], v[192:195]\n"
    "s_waitcnt lgkmcnt(1)\n"
    "v_exp_f32 v204, v204\n"
    "v_mfma_f32_16x16x32_bf16 v[0:3], a[128:131], v[160:163], v[0:3]\n"
    "s_cselect_b32 s60, s59, s72\n"
    "v_exp_f32 v207, v207\n"
    "v_mfma_f32_16x16x32_bf16 v[0:3], a[132:135], v[164:167], v[0:3]\n"
    "s_waitcnt lgkmcnt(0)\n"
    "buffer_store_dwordx4 a[180:183], v235, s[48:51], s74 offen nt\n"
    "v_mfma_f32_16x16x32_bf16 v[4:7], a[128:131], v[168:171], v[4:7]\n"
    "s_cselect_b32 s58, 0, s58\n"
    "v_exp_f32 v206, v206\n"
    "v_mfma_f32_16x16x32_bf16 v[4:7], a[132:135], v[172:175], v[4:7]\n"
    "v_cvt_pk_bf16_f32 v190, v204, v205\n"
    "v_mfma_f32_16x16x32_bf16 v[196:199], a[64:67], a[16:19], v[212:215]\n"
    "ds_read_b128 a[184:187], v234 offset:1024\n"
    "v_mfma_f32_16x16x32_bf16 v[196:199], a[68:71], a[20:23], v[196:199]\n"
    "v_exp_f32 v193, v193\n"
    "v_mfma_f32_16x16x32_bf16 v[196:199], a[72:75], a[24:27], v[196:199]\n"
    "v_exp_f32 v192, v192\n"
    "v_mfma_f32_16x16x32_bf16 v[196:199], a[76:79], a[28:31], v[196:199]\n"
    "v_cvt_pk_bf16_f32 v191, v206, v207\n"
    "v_add_u32 v234, 2048, v234\n"
    "v_mfma_f32_16x16x32_bf16 v[8:11], a[128:131], v[176:179], v[8:11]\n"
    "v_exp_f32 v194, v194\n"
    "v_mfma_f32_16x16x32_bf16 v[8:11], a[132:135], v[180:183], v[8:11]\n"
    "s_waitcnt lgkmcnt(0)\n"
    "buffer_store_dwordx4 a[184:187], v235, s[48:51], s74 offen offset:2048 nt\n"
    "v_mfma_f32_16x16x32_bf16 v[12:15], a[128:131], v[184:187], v[12:15]\n"
    "s_add_u32 s74, s74, 4096\n"
    "ds_read_b128 a[84:87], v224 offset:4352\n"
    "v_mfma_f32_16x16x32_bf16 v[12:15], a[132:135], v[188:191], v[12:15]\n"
    "s_mov_b32 m0, s57\n"
    "v_exp_f32 v195, v195\n"
    "v_mfma_f32_16x16x32_bf16 v[200:203], a[64:67], a[32:35], v[248:251]\n"
    "s_add_u32 s75, s75, 0x40000000\n"
    "ds_read_b64_tr_b16 a[148:149], v225 offset:25088\n"
    "v_mfma_f32_16x16x32_bf16 v[200:203], a[68:71], a[36:39], v[200:203]\n"
    "s_cselect_b32 s71, 16256, 0\n"
    "ds_read_b64_tr_b16 a[152:153], v225 offset:17152\n"
    "ds_read_b64_tr_b16 a[134:135], v225 offset:29696\n"
    "v_mfma_f32_16x16x32_bf16 v[200:203], a[72:75], a[40:43], v[200:203]\n"
    "s_sub_u32 s74, s74, s71\n"
    "ds_read_b64_tr_b16 a[146:147], v225 offset:20992\n"
    "v_mfma_f32_16x16x32_bf16 v[200:203], a[76:79], a[44:47], v[200:203]\n"
    "ds_read_b128 a[88:91], v224 offset:4608\n"
    "buffer_load_dwordx4 v227, s[36:39], 0 offen nt lds\n"
    "v_mfma_f32_16x16x32_bf16 v[16:19], a[136:139], v[160:163], v[16:19]\n"
    "ds_read_b128 a[80:83], v224 offset:4096\n"
    "v_cvt_pk_bf16_f32 v129, v194, v195\n"
    "v_mfma_f32_16x16x32_bf16 v[16:19], a[140:143], v[164:167], v[16:19]\n"
    "ds_read_b128 a[92:95], v224 offset:4864\n"
    "v_mfma_f32_16x16x32_bf16 v[20:23], a[136:139], v[168:171], v[20:23]\n"
    "ds_read_b64_tr_b16 a[150:151], v225 offset:29184\n"
    "v_mfma_f32_16x16x32_bf16 v[20:23], a[140:143], v[172:175], v[20:23]\n"
    "v_exp_f32 v203, v203\n"
    "v_smfmac_f32_16x16x64_bf16 a[160:163], a[176:179], v[160:167], v237\n"
    "ds_read_b64_tr_b16 a[158:159], v225 offset:29440\n"
    "v_mfma_f32_16x16x32_bf16 v[204:207], a[64:67], a[48:51], v[252:255]\n"
    "v_exp_f32 v202, v202\n"
    "v_mfma_f32_16x16x32_bf16 v[204:207], a[68:71], a[52:55], v[204:207]\n"
    "ds_read_b64_tr_b16 a[144:145], v225 offset:16896\n"
    "buffer_load_dwordx4 v227, s[36:39], 0 offen offset:1024 nt lds\n"
    "v_mfma_f32_16x16x32_bf16 v[204:207], a[72:75], a[56:59], v[204:207]\n"
    "v_exp_f32 v196, v196\n"
    "v_mfma_f32_16x16x32_bf16 v[204:207], a[76:79], a[60:63], v[204:207]\n"
    "v_exp_f32 v200, v200\n"
    "v_mfma_f32_16x16x32_bf16 v[24:27], a[136:139], v[176:179], v[24:27]\n"
    "v_exp_f32 v199, v199\n"
    "v_mfma_f32_16x16x32_bf16 v[24:27], a[140:143], v[180:183], v[24:27]\n"
    "v_exp_f32 v198, v198\n"
    "v_mfma_f32_16x16x32_bf16 v[28:31], a[136:139], v[184:187], v[28:31]\n"
    "ds_read_b64_tr_b16 a[130:131], v225 offset:21504\n"
    "v_cvt_pk_bf16_f32 v128, v192, v193\n"
    "v_mfma_f32_16x16x32_bf16 v[28:31], a[140:143], v[188:191], v[28:31]\n"
    "s_waitcnt lgkmcnt(5)\n"
    "ds_read_b64_tr_b16 a[156:157], v225 offset:25344\n"
    "v_cvt_pk_bf16_f32 v145, v202, v203\n"
    "v_mfma_f32_16x16x32_bf16 v[192:195], a[80:83], a[0:3], v[208:211]\n"
    "ds_read_b128 a[64:67], v224 offset:8192\n"
    "buffer_load_dwordx4 v228, s[36:39], 0 offen offset:2048 nt lds\n"
    "v_mfma_f32_16x16x32_bf16 v[192:195], a[84:87], a[4:7], v[192:195]\n"
    "v_exp_f32 v201, v201\n"
    "v_mfma_f32_16x16x32_bf16 v[192:195], a[88:91], a[8:11], v[192:195]\n"
    "s_waitcnt lgkmcnt(6)\n"
    "ds_read_b64_tr_b16 a[154:155], v225 offset:21248\n"
    "ds_read_b64_tr_b16 a[140:141], v225 offset:25856\n"
    "v_mfma_f32_16x16x32_bf16 v[192:195], a[92:95], a[12:15], v[192:195]\n"
    "s_waitcnt lgkmcnt(5)\n"
    "ds_read_b128 a[76:79], v224 offset:8960\n"
    "v_cvt_pk_bf16_f32 v137, v198, v199\n"
    "v_mfma_f32_16x16x32_bf16 v[32:35], a[144:147], v[160:163], v[32:35]\n"
    "v_exp_f32 v204, v204\n"
    "v_mfma_f32_16x16x32_bf16 v[32:35], a[148:151], v[164:167], v[32:35]\n"
    "v_exp_f32 v197, v197\n"
    "v_mfma_f32_16x16x32_bf16 v[36:39], a[144:147], v[168:171], v[36:39]\n"
    "ds_read_b64_tr_b16 a[138:139], v225 offset:21760\n"
    "v_cvt_pk_bf16_f32 v144, v200, v201\n"
    "v_mfma_f32_16x16x32_bf16 v[36:39], a[148:151], v[172:175], v[36:39]\n"
    "buffer_load_dwordx4 v228, s[36:39], 0 offen offset:3072 nt lds\n"
    "v_cvt_pk_bf16_f32 v136, v196, v197\n"
    "v_mfma_f32_16x16x32_bf16 v[196:199], a[80:83], a[16:19], v[212:215]\n"
    "s_add_u32 s52, s52, 16384\n"
    "v_exp_f32 v194, v194\n"
    "v_mfma_f32_16x16x32_bf16 v[196:199], a[84:87], a[20:23], v[196:199]\n"
    "s_add_u32 s54, s54, 0x20000000\n"
    "ds_read_b64_tr_b16 a[142:143], v225 offset:29952\n"
    "v_mfma_f32_16x16x32_bf16 v[196:199], a[88:91], a[24:27], v[196:199]\n"
    "s_cselect_b32 s52, s56, s52\n"
    "v_exp_f32 v193, v193\n"
    "v_mfma_f32_16x16x32_bf16 v[196:199], a[92:95], a[28:31], v[196:199]\n"
    "s_add_u32 s36, s26, s52\n"
    "v_exp_f32 v205, v205\n"
    "v_mfma_f32_16x16x32_bf16 v[40:43], a[144:147], v[176:179], v[40:43]\n"
    "s_addc_u32 s37, s27, 0\n"
    "v_exp_f32 v206, v206\n"
    "v_mfma_f32_16x16x32_bf16 v[40:43], a[148:151], v[180:183], v[40:43]\n"
    "s_cmp_lt_u32 s52, 268435456\n"
    "v_exp_f32 v192, v192\n"
    "v_mfma_f32_16x16x32_bf16 v[44:47], a[144:147], v[184:187], v[44:47]\n"
    "s_cselect_b32 s38, 4096, 0\n"
    "ds_read_b128 a[68:71], v224 offset:8448\n"
    "v_cvt_pk_bf16_f32 v152, v204, v205\n"
    "v_mfma_f32_16x16x32_bf16 v[44:47], a[148:151], v[188:191], v[44:47]\n"
    "s_add_u32 s57, s57, 16384\n"
    "v_exp_f32 v207, v207\n"
    "v_mfma_f32_16x16x32_bf16 v[200:203], a[80:83], a[32:35], v[248:251]\n"
    "s_cmp_ge_u32 s57, s92\n"
    "ds_read_b128 a[72:75], v224 offset:8704\n"
    "v_mfma_f32_16x16x32_bf16 v[200:203], a[84:87], a[36:39], v[200:203]\n"
    "s_cselect_b32 s71, 49152, 0\n"
    "v_exp_f32 v197, v197\n"
    "v_mfma_f32_16x16x32_bf16 v[200:203], a[88:91], a[40:43], v[200:203]\n"
    "s_sub_u32 s57, s57, s71\n"
    "v_exp_f32 v196, v196\n"
    "v_mfma_f32_16x16x32_bf16 v[200:203], a[92:95], a[44:47], v[200:203]\n"
    "s_waitcnt lgkmcnt(6)\n"
    "ds_read_b64_tr_b16 a[146:147], v225 offset:22016\n"
    "v_add_u32 v243, 1, v243\n"
    "v_mfma_f32_16x16x32_bf16 v[48:51], a[152:155], v[160:163], v[48:51]\n"
    "s_add_u32 s8, s8, s8\n"
    "ds_read_b64_tr_b16 a[132:133], v225 offset:25600\n"
    "v_mfma_f32_16x16x32_bf16 v[48:51], a[156:159], v[164:167], v[48:51]\n"
    "s_cselect_b32 s46, s9, 0\n"
    "ds_read_b64_tr_b16 a[128:129], v225 offset:17408\n"
    "v_cvt_pk_bf16_f32 v130, v192, v193\n"
    "v_mfma_f32_16x16x32_bf16 v[52:55], a[152:155], v[168:171], v[52:55]\n"
    "s_cselect_b32 s10, 2048, 0\n"
    "v_cvt_pk_bf16_f32 v153, v206, v207\n"
    "v_mfma_f32_16x16x32_bf16 v[52:55], a[156:159], v[172:175], v[52:55]\n"
    "s_cselect_b32 m0, s73, 148480\n"
    "v_exp_f32 v195, v195\n"
    "v_smfmac_f32_16x16x64_bf16 a[164:167], a[176:179], v[168:175], v237\n"
    "v_cvt_pk_bf16_f32 v131, v194, v195\n"
    "v_mfma_f32_16x16x32_bf16 v[204:207], a[80:83], a[48:51], v[252:255]\n"
    "ds_read_b64_tr_b16 a[144:145], v225 offset:17920\n"
    "ds_read_b64_tr_b16 a[150:151], v225 offset:30208\n"
    "v_mfma_f32_16x16x32_bf16 v[204:207], a[84:87], a[52:55], v[204:207]\n"
    "v_exp_f32 v199, v199\n"
    "v_mfma_f32_16x16x32_bf16 v[204:207], a[88:91], a[56:59], v[204:207]\n"
    "buffer_load_dwordx4 v223, s[44:47], 0 offen nt lds\n"
    "v_mfma_f32_16x16x32_bf16 v[204:207], a[92:95], a[60:63], v[204:207]\n"
    "v_exp_f32 v201, v201\n"
    "v_mfma_f32_16x16x32_bf16 v[56:59], a[152:155], v[176:179], v[56:59]\n"
    "ds_read_b128 a[80:83], v224 offset:12288\n"
    "ds_read_b128 a[92:95], v224 offset:13056\n"
    "v_mfma_f32_16x16x32_bf16 v[56:59], a[156:159], v[180:183], v[56:59]\n"
    "v_exp_f32 v198, v198\n"
    "v_mfma_f32_16x16x32_bf16 v[60:63], a[152:155], v[184:187], v[60:63]\n"
    "v_exp_f32 v200, v200\n"
    "v_mfma_f32_16x16x32_bf16 v[60:63], a[156:159], v[188:191], v[60:63]\n"
    "ds_read_b64_tr_b16 a[152:153], v225 offset:18176\n"
    "buffer_load_dwordx4 v223, s[44:47], 0 offen offset:1024 nt lds\n"
    "v_mfma_f32_16x16x32_bf16 v[192:195], a[64:67], a[0:3], v[208:211]\n"
    "s_waitcnt lgkmcnt(9)\n"
    "s_add_u32 s8, s8, s8\n"
    "v_exp_f32 v203, v203\n"
    "v_mfma_f32_16x16x32_bf16 v[192:195], a[68:71], a[4:7], v[192:195]\n"
    "s_waitcnt lgkmcnt(8)\n"
    "ds_read_b64_tr_b16 a[156:157], v225 offset:26368\n"
    "v_cvt_pk_bf16_f32 v139, v198, v199\n"
    "v_mfma_f32_16x16x32_bf16 v[192:195], a[72:75], a[8:11], v[192:195]\n"
    "s_cselect_b32 s46, s9, 0\n"
    "ds_read_b128 a[84:87], v224 offset:12544\n"
    "ds_read_b64_tr_b16 a[136:137], v225 offset:17664\n"
    "v_mfma_f32_16x16x32_bf16 v[192:195], a[76:79], a[12:15], v[192:195]\n"
    "s_waitcnt lgkmcnt(8)\n"
    "s_cselect_b32 s11, 2048, 0\n"
    "ds_read_b64_tr_b16 a[148:149], v225 offset:26112\n"
    "ds_read_b64_tr_b16 a[154:155], v225 offset:22272\n"
    "v_mfma_f32_16x16x32_bf16 v[64:67], a[128:131], v[160:163], v[64:67]\n"
    "s_cselect_b32 m0, s73, 148480\n"
    "v_exp_f32 v205, v205\n"
    "v_mfma_f32_16x16x32_bf16 v[64:67], a[132:135], v[164:167], v[64:67]\n"
    "s_add_u32 s10, s10, s11\n"
    "ds_read_b64_tr_b16 a[158:159], v225 offset:30464\n"
    "buffer_load_dwordx4 v228, s[44:47], 0 offen offset:2048 nt lds\n"
    "v_mfma_f32_16x16x32_bf16 v[68:71], a[128:131], v[168:171], v[68:71]\n"
    "s_cmp_eq_u32 s10, 2048\n"
    "ds_read_b128 a[88:91], v224 offset:12800\n"
    "v_cvt_pk_bf16_f32 v138, v196, v197\n"
    "v_mfma_f32_16x16x32_bf16 v[68:71], a[132:135], v[172:175], v[68:71]\n"
    "s_waitcnt vmcnt(9)\n"
    "ds_write_b32 v240, v243\n"
    "v_mfma_f32_16x16x32_bf16 v[196:199], a[64:67], a[16:19], v[212:215]\n"
    "s_cselect_b32 s12, 48, 0\n"
    "v_exp_f32 v202, v202\n"
    "v_mfma_f32_16x16x32_bf16 v[196:199], a[68:71], a[20:23], v[196:199]\n"
    "v_exp_f32 v204, v204\n"
    "v_mfma_f32_16x16x32_bf16 v[196:199], a[72:75], a[24:27], v[196:199]\n"
    "buffer_load_dwordx4 v228, s[44:47], 0 offen offset:3072 nt lds\n"
    "v_cvt_pk_bf16_f32 v146, v200, v201\n"
    "v_mfma_f32_16x16x32_bf16 v[196:199], a[76:79], a[28:31], v[196:199]\n"
    "s_add_u32 s73, s73, s10\n"
    "v_cvt_pk_bf16_f32 v147, v202, v203\n"
    "v_mfma_f32_16x16x32_bf16 v[72:75], a[128:131], v[176:179], v[72:75]\n"
    "s_add_u32 s44, s44, s10\n"
    "v_exp_f32 v207, v207\n"
    "v_mfma_f32_16x16x32_bf16 v[72:75], a[132:135], v[180:183], v[72:75]\n"
    "s_addc_u32 s45, s45, 0\n"
    "v_exp_f32 v206, v206\n"
    "v_mfma_f32_16x16x32_bf16 v[76:79], a[128:131], v[184:187], v[76:79]\n"
    "s_sub_i32 s9, s9, s10\n"
    "v_cvt_pk_bf16_f32 v154, v204, v205\n"
    "v_mfma_f32_16x16x32_bf16 v[76:79], a[132:135], v[188:191], v[76:79]\n"
    "s_max_i32 s9, s9, 0\n"
    "v_exp_f32 v198, v198\n"
    "v_mfma_f32_16x16x32_bf16 v[200:203], a[64:67], a[32:35], v[248:251]\n"
    "s_add_u32 s53, s53, 16384\n"
    "v_cvt_pk_bf16_f32 v155, v206, v207\n"
    "v_mfma_f32_16x16x32_bf16 v[200:203], a[68:71], a[36:39], v[200:203]\n"
    "s_add_u32 s55, s55, 0x20000000\n"
    "v_exp_f32 v199, v199\n"
    "v_mfma_f32_16x16x32_bf16 v[200:203], a[72:75], a[40:43], v[200:203]\n"
    "s_cselect_b32 s53, s56, s53\n"
    "v_mfma_f32_16x16x32_bf16 v[200:203], a[76:79], a[44:47], v[200:203]\n"
    "s_waitcnt lgkmcnt(5)\n"
    "v_exp_f32 v193, v193\n"
    "v_mfma_f32_16x16x32_bf16 v[80:83], a[136:139], v[160:163], v[80:83]\n"
    "s_add_u32 s40, s28, s53\n"
    "v_mfma_f32_16x16x32_bf16 v[80:83], a[140:143], v[164:167], v[80:83]\n"
    "s_addc_u32 s41, s29, 0\n"
    "v_exp_f32 v194, v194\n"
    "v_mfma_f32_16x16x32_bf16 v[84:87], a[136:139], v[168:171], v[84:87]\n"
    "s_cmp_lt_u32 s53, 268435456\n"
    "v_exp_f32 v195, v195\n"
    "v_mfma_f32_16x16x32_bf16 v[84:87], a[140:143], v[172:175], v[84:87]\n"
    "s_cselect_b32 s42, 4096, 0\n"
    "v_exp_f32 v192, v192\n"
    "v_smfmac_f32_16x16x64_bf16 a[168:171], a[176:179], v[176:183], v237\n"
    "v_add_u32 v224, s60, v224\n"
    "v_mfma_f32_16x16x32_bf16 v[204:207], a[64:67], a[48:51], v[252:255]\n"
    "v_exp_f32 v196, v196\n"
    "v_mfma_f32_16x16x32_bf16 v[204:207], a[68:71], a[52:55], v[204:207]\n"
    "ds_read_b32 v242, v241\n"
    "v_cvt_pk_bf16_f32 v133, v194, v195\n"
    "v_mfma_f32_16x16x32_bf16 v[204:207], a[72:75], a[56:59], v[204:207]\n"
    "v_exp_f32 v197, v197\n"
    "v_mfma_f32_16x16x32_bf16 v[204:207], a[76:79], a[60:63], v[204:207]\n"
    "v_cvt_pk_bf16_f32 v132, v192, v193\n"
    "v_mfma_f32_16x16x32_bf16 v[88:91], a[136:139], v[176:179], v[88:91]\n"
    "v_exp_f32 v203, v203\n"
    "v_mfma_f32_16x16x32_bf16 v[88:91], a[140:143], v[180:183], v[88:91]\n"
    "v_mfma_f32_16x16x32_bf16 v[92:95], a[136:139], v[184:187], v[92:95]\n"
    "v_exp_f32 v202, v202\n"
    "v_mfma_f32_16x16x32_bf16 v[92:95], a[140:143], v[188:191], v[92:95]\n"
    "s_waitcnt lgkmcnt(0)\n"
    "v_cvt_pk_bf16_f32 v140, v196, v197\n"
    "v_cmp_gt_u32 vcc, v243, v242\n"
    "v_mfma_f32_16x16x32_bf16 v[192:195], a[80:83], a[0:3], v[208:211]\n"
    "s_cbranch_vccnz LSPTo%=\n"
    "LSRTo%=:\n"
    "v_exp_f32 v204, v204\n"
    "v_mfma_f32_16x16x32_bf16 v[192:195], a[84:87], a[4:7], v[192:195]\n"
    "s_add_u32 m0, s80, 16384\n"
    "v_exp_f32 v207, v207\n"
    "v_mfma_f32_16x16x32_bf16 v[192:195], a[88:91], a[8:11], v[192:195]\n"
    "ds_read_b64_tr_b16 a[140:141], v225 offset:8448\n"
    "v_mfma_f32_16x16x32_bf16 v[192:195], a[92:95], a[12:15], v[192:195]\n"
    "buffer_load_dwordx4 v229, s[40:43], 0 offen nt lds\n"
    "v_cvt_pk_bf16_f32 v141, v198, v199\n"
    "v_mfma_f32_16x16x32_bf16 v[96:99], a[144:147], v[160:163], v[96:99]\n"
    "v_exp_f32 v200, v200\n"
    "v_mfma_f32_16x16x32_bf16 v[96:99], a[148:151], v[164:167], v[96:99]\n"
    "ds_read_b128 a[68:71], v224 offset:256\n"
    "v_xor_b32 v223, s12, v223\n"
    "v_mfma_f32_16x16x32_bf16 v[100:103], a[144:147], v[168:171], v[100:103]\n"
    "ds_read_b128 a[76:79], v224 offset:768\n"
    "ds_read_b64_tr_b16 a[128:129], v225 offset:0\n"
    "v_mfma_f32_16x16x32_bf16 v[100:103], a[148:151], v[172:175], v[100:103]\n"
    "v_exp_f32 v206, v206\n"
    "v_mfma_f32_16x16x32_bf16 v[196:199], a[80:83], a[16:19], v[212:215]\n"
    "v_exp_f32 v205, v205\n"
    "v_mfma_f32_16x16x32_bf16 v[196:199], a[84:87], a[20:23], v[196:199]\n"
    "ds_read_b64_tr_b16 a[130:131], v225 offset:4096\n"
    "v_cvt_pk_bf16_f32 v157, v206, v207\n"
    "v_mfma_f32_16x16x32_bf16 v[196:199], a[88:91], a[24:27], v[196:199]\n"
    "v_exp_f32 v194, v194\n"
    "v_mfma_f32_16x16x32_bf16 v[196:199], a[92:95], a[28:31], v[196:199]\n"
    "buffer_load_dwordx4 v229, s[40:43], 0 offen offset:2048 nt lds\n"
    "v_cvt_pk_bf16_f32 v149, v202, v203\n"
    "v_mfma_f32_16x16x32_bf16 v[104:107], a[144:147], v[176:179], v[104:107]\n"
    "s_add_u32 m0, s80, 17280\n"
    "v_exp_f32 v201, v201\n"
    "v_mfma_f32_16x16x32_bf16 v[104:107], a[148:151], v[180:183], v[104:107]\n"
    "v_cvt_pk_bf16_f32 v148, v200, v201\n"
    "v_cvt_pk_bf16_f32 v156, v204, v205\n"
    "v_mfma_f32_16x16x32_bf16 v[108:111], a[144:147], v[184:187], v[108:111]\n"
    "v_exp_f32 v193, v193\n"
    "v_mfma_f32_16x16x32_bf16 v[108:111], a[148:151], v[188:191], v[108:111]\n"
    "v_exp_f32 v195, v195\n"
    "v_mfma_f32_16x16x32_bf16 v[200:203], a[80:83], a[32:35], v[248:251]\n"
    "v_exp_f32 v198, v198\n"
    "v_mfma_f32_16x16x32_bf16 v[200:203], a[84:87], a[36:39], v[200:203]\n"
    "v_exp_f32 v197, v197\n"
    "v_mfma_f32_16x16x32_bf16 v[200:203], a[88:91], a[40:43], v[200:203]\n"
    "v_exp_f32 v199, v199\n"
    "v_mfma_f32_16x16x32_bf16 v[200:203], a[92:95], a[44:47], v[200:203]\n"
    "buffer_load_dwordx4 v229, s[40:43], 0 offen offset:128 nt lds\n"
    "ds_read_b64_tr_b16 a[136:137], v225 offset:256\n"
    "v_mfma_f32_16x16x32_bf16 v[112:115], a[152:155], v[160:163], v[112:115]\n"
    "v_exp_f32 v192, v192\n"
    "v_mfma_f32_16x16x32_bf16 v[112:115], a[156:159], v[164:167], v[112:115]\n"
    "v_exp_f32 v196, v196\n"
    "v_mfma_f32_16x16x32_bf16 v[116:119], a[152:155], v[168:171], v[116:119]\n"
    "ds_read_b128 a[72:75], v224 offset:512\n"
    "ds_read_b64_tr_b16 a[134:135], v225 offset:12288\n"
    "v_mfma_f32_16x16x32_bf16 v[116:119], a[156:159], v[172:175], v[116:119]\n"
    "ds_read_b128 a[64:67], v224 offset:0\n"
    "ds_read_b64_tr_b16 a[138:139], v225 offset:4352\n"
    "v_smfmac_f32_16x16x64_bf16 a[172:175], a[176:179], v[184:191], v237\n"
    "v_cvt_pk_bf16_f32 v134, v192, v193\n"
    "v_mfma_f32_16x16x32_bf16 v[204:207], a[80:83], a[48:51], v[252:255]\n"
    "v_exp_f32 v200, v200\n"
    "v_mfma_f32_16x16x32_bf16 v[204:207], a[84:87], a[52:55], v[204:207]\n"
    "ds_read_b64_tr_b16 a[132:133], v225 offset:8192\n"
    "v_cvt_pk_bf16_f32 v135, v194, v195\n"
    "v_mfma_f32_16x16x32_bf16 v[204:207], a[88:91], a[56:59], v[204:207]\n"
    "buffer_load_dwordx4 v229, s[40:43], 0 offen offset:2176 nt lds\n"
    "v_cvt_pk_bf16_f32 v142, v196, v197\n"
    "v_mfma_f32_16x16x32_bf16 v[204:207], a[92:95], a[60:63], v[204:207]\n"
    "s_add_u32 s70, s70, 1\n"
    "v_exp_f32 v203, v203\n"
    "v_mfma_f32_16x16x32_bf16 v[120:123], a[152:155], v[176:179], v[120:123]\n"
    "v_exp_f32 v202, v202\n"
    "v_mfma_f32_16x16x32_bf16 v[120:123], a[156:159], v[180:183], v[120:123]\n"
    "v_cvt_pk_bf16_f32 v143, v198, v199\n"
    "v_cvt_pk_bf16_f32 v151, v202, v203\n"
    "v_mfma_f32_16x16x32_bf16 v[124:127], a[152:155], v[184:187], v[124:127]\n"
    "v_exp_f32 v201, v201\n"
    "v_mfma_f32_16x16x32_bf16 v[124:127], a[156:159], v[188:191], v[124:127]\n"
    "s_waitcnt lgkmcnt(2)\n"
    "ds_read_b64_tr_b16 a[142:143], v225 offset:12544\n"
    "v_cvt_pk_bf16_f32 v150, v200, v201\n"
    "v_mfma_f32_16x16x32_bf16 v[192:195], a[64:67], a[0:3], v[208:211]\n"
    "ds_read_b128 a[92:95], v224 offset:4864\n"
    "v_mfma_f32_16x16x32_bf16 v[192:195], a[68:71], a[4:7], v[192:195]\n"
    "s_add_u32 s58, s58, 16384\n"
    "ds_read_b128 a[180:183], v234\n"
    "v_mfma_f32_16x16x32_bf16 v[192:195], a[72:75], a[8:11], v[192:195]\n"
    "s_cmp_eq_u32 s58, 49152\n"
    "v_exp_f32 v205, v205\n"
    "v_mfma_f32_16x16x32_bf16 v[192:195], a[76:79], a[12:15], v[192:195]\n"
    "s_cselect_b32 s60, s59, s72\n"
    "v_exp_f32 v206, v206\n"
    "v_mfma_f32_16x16x32_bf16 v[0:3], a[128:131], v[128:131], v[0:3]\n"
    "s_waitcnt lgkmcnt(3)\n"
    "v_exp_f32 v207, v207\n"
    "v_mfma_f32_16x16x32_bf16 v[0:3], a[132:135], v[132:135], v[0:3]\n"
    "s_waitcnt lgkmcnt(0)\n"
    "buffer_store_dwordx4 a[180:183], v235, s[48:51], s74 offen nt\n"
    "v_mfma_f32_16x16x32_bf16 v[4:7], a[128:131], v[136:139], v[4:7]\n"
    "s_cselect_b32 s58, 0, s58\n"
    "v_exp_f32 v204, v204\n"
    "v_mfma_f32_16x16x32_bf16 v[4:7], a[132:135], v[140:143], v[4:7]\n"
    "v_mfma_f32_16x16x32_bf16 v[196:199], a[64:67], a[16:19], v[212:215]\n"
    "ds_read_b128 a[184:187], v234 offset:1024\n"
    "v_cvt_pk_bf16_f32 v159, v206, v207\n"
    "v_mfma_f32_16x16x32_bf16 v[196:199], a[68:71], a[20:23], v[196:199]\n"
    "ds_read_b64_tr_b16 a[148:149], v225 offset:8704\n"
    "v_cvt_pk_bf16_f32 v158, v204, v205\n"
    "v_mfma_f32_16x16x32_bf16 v[196:199], a[72:75], a[24:27], v[196:199]\n"
    "v_exp_f32 v194, v194\n"
    "v_mfma_f32_16x16x32_bf16 v[196:199], a[76:79], a[28:31], v[196:199]\n"
    "ds_read_b64_tr_b16 a[152:153], v225 offset:768\n"
    "v_add_u32 v234, 2048, v234\n"
    "v_mfma_f32_16x16x32_bf16 v[8:11], a[128:131], v[144:147], v[8:11]\n"
    "ds_read_b64_tr_b16 a[144:145], v225 offset:512\n"
    "ds_read_b64_tr_b16 a[154:155], v225 offset:4864\n"
    "v_mfma_f32_16x16x32_bf16 v[8:11], a[132:135], v[148:151], v[8:11]\n"
    "s_waitcnt lgkmcnt(4)\n"
    "buffer_store_dwordx4 a[184:187], v235, s[48:51], s74 offen offset:2048 nt\n"
    "v_mfma_f32_16x16x32_bf16 v[12:15], a[128:131], v[152:155], v[12:15]\n"
    "s_add_u32 s74, s74, 4096\n"
    "v_exp_f32 v195, v195\n"
    "v_mfma_f32_16x16x32_bf16 v[12:15], a[132:135], v[156:159], v[12:15]\n"
    "s_mov_b32 m0, s57\n"
    "ds_read_b128 a[88:91], v224 offset:4608\n"
    "ds_read_b64_tr_b16 a[150:151], v225 offset:12800\n"
    "v_mfma_f32_16x16x32_bf16 v[200:203], a[64:67], a[32:35], v[248:251]\n"
    "s_add_u32 s75, s75, 0x40000000\n"
    "v_exp_f32 v192, v192\n"
    "v_mfma_f32_16x16x32_bf16 v[200:203], a[68:71], a[36:39], v[200:203]\n"
    "s_cselect_b32 s71, 16256, 0\n"
    "ds_read_b128 a[80:83], v224 offset:4096\n"
    "ds_read_b64_tr_b16 a[130:131], v225 offset:5120\n"
    "v_mfma_f32_16x16x32_bf16 v[200:203], a[72:75], a[40:43], v[200:203]\n"
    "s_sub_u32 s74, s74, s71\n"
    "v_cvt_pk_bf16_f32 v161, v194, v195\n"
    "v_mfma_f32_16x16x32_bf16 v[200:203], a[76:79], a[44:47], v[200:203]\n"
    "ds_read_b64_tr_b16 a[146:147], v225 offset:4608\n"
    "buffer_load_dwordx4 v227, s[36:39], 0 offen nt lds\n"
    "v_mfma_f32_16x16x32_bf16 v[16:19], a[136:139], v[128:131], v[16:19]\n"
    "v_exp_f32 v198, v198\n"
    "v_mfma_f32_16x16x32_bf16 v[16:19], a[140:143], v[132:135], v[16:19]\n"
    "v_exp_f32 v197, v197\n"
    "v_mfma_f32_16x16x32_bf16 v[20:23], a[136:139], v[136:139], v[20:23]\n"
    "v_exp_f32 v196, v196\n"
    "v_mfma_f32_16x16x32_bf16 v[20:23], a[140:143], v[140:143], v[20:23]\n"
    "v_smfmac_f32_16x16x64_bf16 a[160:163], a[176:179], v[128:135], v237\n"
    "ds_read_b128 a[84:87], v224 offset:4352\n"
    "v_mfma_f32_16x16x32_bf16 v[204:207], a[64:67], a[48:51], v[252:255]\n"
    "ds_read_b64_tr_b16 a[132:133], v225 offset:9216\n"
    "v_cvt_pk_bf16_f32 v168, v196, v197\n"
    "v_mfma_f32_16x16x32_bf16 v[204:207], a[68:71], a[52:55], v[204:207]\n"
    "buffer_load_dwordx4 v227, s[36:39], 0 offen offset:1024 nt lds\n"
    "v_mfma_f32_16x16x32_bf16 v[204:207], a[72:75], a[56:59], v[204:207]\n"
    "v_exp_f32 v201, v201\n"
    "v_mfma_f32_16x16x32_bf16 v[204:207], a[76:79], a[60:63], v[204:207]\n"
    "v_exp_f32 v199, v199\n"
    "v_mfma_f32_16x16x32_bf16 v[24:27], a[136:139], v[144:147], v[24:27]\n"
    "v_exp_f32 v202, v202\n"
    "v_mfma_f32_16x16x32_bf16 v[24:27], a[140:143], v[148:151], v[24:27]\n"
    "v_exp_f32 v193, v193\n"
    "v_mfma_f32_16x16x32_bf16 v[28:31], a[136:139], v[152:155], v[28:31]\n"
    "v_cvt_pk_bf16_f32 v160, v192, v193\n"
    "v_mfma_f32_16x16x32_bf16 v[28:31], a[140:143], v[156:159], v[28:31]\n"
    "s_waitcnt lgkmcnt(4)\n"
    "v_exp_f32 v203, v203\n"
    "v_mfma_f32_16x16x32_bf16 v[192:195], a[80:83], a[0:3], v[208:211]\n"
    "s_waitcnt lgkmcnt(1)\n"
    "ds_read_b64_tr_b16 a[158:159], v225 offset:13056\n"
    "buffer_load_dwordx4 v228, s[36:39], 0 offen offset:2048 nt lds\n"
    "v_mfma_f32_16x16x32_bf16 v[192:195], a[84:87], a[4:7], v[192:195]\n"
    "v_exp_f32 v200, v200\n"
    "v_mfma_f32_16x16x32_bf16 v[192:195], a[88:91], a[8:11], v[192:195]\n"
    "ds_read_b128 a[76:79], v224 offset:8960\n"
    "v_cvt_pk_bf16_f32 v176, v200, v201\n"
    "v_mfma_f32_16x16x32_bf16 v[192:195], a[92:95], a[12:15], v[192:195]\n"
    "v_exp_f32 v205, v205\n"
    "v_mfma_f32_16x16x32_bf16 v[32:35], a[144:147], v[128:131], v[32:35]\n"
    "v_cvt_pk_bf16_f32 v169, v198, v199\n"
    "v_cvt_pk_bf16_f32 v177, v202, v203\n"
    "v_mfma_f32_16x16x32_bf16 v[32:35], a[148:151], v[132:135], v[32:35]\n"
    "ds_read_b128 a[68:71], v224 offset:8448\n"
    "v_mfma_f32_16x16x32_bf16 v[36:39], a[144:147], v[136:139], v[36:39]\n"
    "v_exp_f32 v204, v204\n"
    "v_mfma_f32_16x16x32_bf16 v[36:39], a[148:151], v[140:143], v[36:39]\n"
    "ds_read_b64_tr_b16 a[134:135], v225 offset:13312\n"
    "buffer_load_dwordx4 v228, s[36:39], 0 offen offset:3072 nt lds\n"
    "v_mfma_f32_16x16x32_bf16 v[196:199], a[80:83], a[16:19], v[212:215]\n"
    "s_add_u32 s52, s52, 16384\n"
    "v_exp_f32 v193, v193\n"
    "v_mfma_f32_16x16x32_bf16 v[196:199], a[84:87], a[20:23], v[196:199]\n"
    "s_add_u32 s54, s54, 0x20000000\n"
    "v_exp_f32 v192, v192\n"
    "v_mfma_f32_16x16x32_bf16 v[196:199], a[88:91], a[24:27], v[196:199]\n"
    "s_cselect_b32 s52, s56, s52\n"
    "v_exp_f32 v207, v207\n"
    "v_mfma_f32_16x16x32_bf16 v[196:199], a[92:95], a[28:31], v[196:199]\n"
    "s_add_u32 s36, s26, s52\n"
    "ds_read_b128 a[72:75], v224 offset:8704\n"
    "v_cvt_pk_bf16_f32 v184, v204, v205\n"
    "v_mfma_f32_16x16x32_bf16 v[40:43], a[144:147], v[144:147], v[40:43]\n"
    "s_addc_u32 s37, s27, 0\n"
    "v_exp_f32 v206, v206\n"
    "v_mfma_f32_16x16x32_bf16 v[40:43], a[148:151], v[148:151], v[40:43]\n"
    "s_cmp_lt_u32 s52, 268435456\n"
    "ds_read_b64_tr_b16 a[156:157], v225 offset:8960\n"
    "ds_read_b64_tr_b16 a[136:137], v225 offset:1280\n"
    "v_mfma_f32_16x16x32_bf16 v[44:47], a[144:147], v[152:155], v[44:47]\n"
    "s_cselect_b32 s38, 4096, 0\n"
    "v_cvt_pk_bf16_f32 v162, v192, v193\n"
    "v_mfma_f32_16x16x32_bf16 v[44:47], a[148:151], v[156:159], v[44:47]\n"
    "s_add_u32 s57, s57, 16384\n"
    "v_exp_f32 v195, v195\n"
    "v_mfma_f32_16x16x32_bf16 v[200:203], a[80:83], a[32:35], v[248:251]\n"
    "s_cmp_ge_u32 s57, s92\n"
    "ds_read_b64_tr_b16 a[128:129], v225 offset:1024\n"
    "v_mfma_f32_16x16x32_bf16 v[200:203], a[84:87], a[36:39], v[200:203]\n"
    "s_cselect_b32 s71, 49152, 0\n"
    "ds_read_b64_tr_b16 a[144:145], v225 offset:1536\n"
    "ds_read_b64_tr_b16 a[148:149], v225 offset:9728\n"
    "v_mfma_f32_16x16x32_bf16 v[200:203], a[88:91], a[40:43], v[200:203]\n"
    "s_sub_u32 s57, s57, s71\n"
    "ds_read_b64_tr_b16 a[142:143], v225 offset:13568\n"
    "v_mfma_f32_16x16x32_bf16 v[200:203], a[92:95], a[44:47], v[200:203]\n"
    "s_add_u32 s8, s8, s8\n"
    "v_exp_f32 v199, v199\n"
    "v_mfma_f32_16x16x32_bf16 v[48:51], a[152:155], v[128:131], v[48:51]\n"
    "s_waitcnt lgkmcnt(5)\n"
    "v_exp_f32 v196, v196\n"
    "v_mfma_f32_16x16x32_bf16 v[48:51], a[156:159], v[132:135], v[48:51]\n"
    "s_cselect_b32 s46, s9, 0\n"
    "v_exp_f32 v194, v194\n"
    "v_mfma_f32_16x16x32_bf16 v[52:55], a[152:155], v[136:139], v[52:55]\n"
    "s_cselect_b32 s10, 2048, 0\n"
    "ds_read_b64_tr_b16 a[146:147], v225 offset:5632\n"
    "v_cvt_pk_bf16_f32 v185, v206, v207\n"
    "v_mfma_f32_16x16x32_bf16 v[52:55], a[156:159], v[140:143], v[52:55]\n"
    "s_cselect_b32 m0, s73, 148480\n"
    "ds_read_b128 a[64:67], v224 offset:8192\n"
    "v_smfmac_f32_16x16x64_bf16 a[164:167], a[176:179], v[136:143], v237\n"
    "ds_read_b64_tr_b16 a[150:151], v225 offset:13824\n"
    "v_mfma_f32_16x16x32_bf16 v[204:207], a[80:83], a[48:51], v[252:255]\n"
    "ds_read_b64_tr_b16 a[138:139], v225 offset:5376\n"
    "ds_read_b64_tr_b16 a[140:141], v225 offset:9472\n"
    "v_mfma_f32_16x16x32_bf16 v[204:207], a[84:87], a[52:55], v[204:207]\n"
    "v_exp_f32 v197, v197\n"
    "v_mfma_f32_16x16x32_bf16 v[204:207], a[88:91], a[56:59], v[204:207]\n"
    "buffer_load_dwordx4 v223, s[44:47], 0 offen nt lds\n"
    "v_mfma_f32_16x16x32_bf16 v[204:207], a[92:95], a[60:63], v[204:207]\n"
    "v_exp_f32 v198, v198\n"
    "v_mfma_f32_16x16x32_bf16 v[56:59], a[152:155], v[144:147], v[56:59]\n"
    "v_exp_f32 v202, v202\n"
    "v_mfma_f32_16x16x32_bf16 v[56:59], a[156:159], v[148:151], v[56:59]\n"
    "v_exp_f32 v200, v200\n"
    "v_mfma_f32_16x16x32_bf16 v[60:63], a[152:155], v[152:155], v[60:63]\n"
    "v_cvt_pk_bf16_f32 v163, v194, v195\n"
    "v_add_u32 v243, 1, v243\n"
    "v_mfma_f32_16x16x32_bf16 v[60:63], a[156:159], v[156:159], v[60:63]\n"
    "s_waitcnt lgkmcnt(3)\n"
    "ds_read_b64_tr_b16 a[154:155], v225 offset:5888\n"
    "buffer_load_dwordx4 v223, s[44:47], 0 offen offset:1024 nt lds\n"
    "v_mfma_f32_16x16x32_bf16 v[192:195], a[64:67], a[0:3], v[208:211]\n"
    "s_add_u32 s8, s8, s8\n"
    "ds_read_b128 a[80:83], v224 offset:12288\n"
    "ds_read_b128 a[88:91], v224 offset:12800\n"
    "v_mfma_f32_16x16x32_bf16 v[192:195], a[68:71], a[4:7], v[192:195]\n"
    "s_cselect_b32 s46, s9, 0\n"
    "ds_read_b128 a[92:95], v224 offset:13056\n"
    "ds_read_b64_tr_b16 a[152:153], v225 offset:1792\n"
    "v_mfma_f32_16x16x32_bf16 v[192:195], a[72:75], a[8:11], v[192:195]\n"
    "s_cselect_b32 s11, 2048, 0\n"
    "v_exp_f32 v203, v203\n"
    "v_mfma_f32_16x16x32_bf16 v[192:195], a[76:79], a[12:15], v[192:195]\n"
    "s_cselect_b32 m0, s73, 148480\n"
    "ds_read_b64_tr_b16 a[156:157], v225 offset:9984\n"
    "ds_read_b64_tr_b16 a[158:159], v225 offset:14080\n"
    "v_mfma_f32_16x16x32_bf16 v[64:67], a[128:131], v[128:131], v[64:67]\n"
    "s_add_u32 s10, s10, s11\n"
    "v_exp_f32 v205, v205\n"
    "v_mfma_f32_16x16x32_bf16 v[64:67], a[132:135], v[132:135], v[64:67]\n"
    "s_cmp_eq_u32 s10, 2048\n"
    "ds_read_b128 a[84:87], v224 offset:12544\n"
    "buffer_load_dwordx4 v228, s[44:47], 0 offen offset:2048 nt lds\n"
    "v_mfma_f32_16x16x32_bf16 v[68:71], a[128:131], v[136:139], v[68:71]\n"
    "s_cselect_b32 s12, 48, 0\n"
    "v_cvt_pk_bf16_f32 v170, v196, v197\n"
    "v_cvt_pk_bf16_f32 v171, v198, v199\n"
    "v_mfma_f32_16x16x32_bf16 v[68:71], a[132:135], v[140:143], v[68:71]\n"
    "s_waitcnt vmcnt(9)\n"
    "ds_write_b32 v240, v243\n"
    "v_mfma_f32_16x16x32_bf16 v[196:199], a[64:67], a[16:19], v[212:215]\n"
    "v_exp_f32 v192, v192\n"
    "v_mfma_f32_16x16x32_bf16 v[196:199], a[68:71], a[20:23], v[196:199]\n"
    "v_exp_f32 v204, v204\n"
    "v_mfma_f32_16x16x32_bf16 v[196:199], a[72:75], a[24:27], v[196:199]\n"
    "buffer_load_dwordx4 v228, s[44:47], 0 offen offset:3072 nt lds\n"
    "v_cvt_pk_bf16_f32 v179, v202, v203\n"
    "v_mfma_f32_16x16x32_bf16 v[196:199], a[76:79], a[28:31], v[196:199]\n"
    "s_add_u32 s73, s73, s10\n"
    "v_exp_f32 v194, v194\n"
    "v_mfma_f32_16x16x32_bf16 v[72:75], a[128:131], v[144:147], v[72:75]\n"
    "s_add_u32 s44, s44, s10\n"
    "v_exp_f32 v193, v193\n"
    "v_mfma_f32_16x16x32_bf16 v[72:75], a[132:135], v[148:151], v[72:75]\n"
    "s_addc_u32 s45, s45, 0\n"
    "v_exp_f32 v201, v201\n"
    "v_mfma_f32_16x16x32_bf16 v[76:79], a[128:131], v[152:155], v[76:79]\n"
    "s_sub_i32 s9, s9, s10\n"
    "v_cvt_pk_bf16_f32 v178, v200, v201\n"
    "v_cvt_pk_bf16_f32 v186, v204, v205\n"
    "v_mfma_f32_16x16x32_bf16 v[76:79], a[132:135], v[156:159], v[76:79]\n"
    "s_max_i32 s9, s9, 0\n"
    "v_exp_f32 v198, v198\n"
    "v_mfma_f32_16x16x32_bf16 v[200:203], a[64:67], a[32:35], v[248:251]\n"
    "s_add_u32 s53, s53, 16384\n"
    "v_exp_f32 v206, v206\n"
    "v_mfma_f32_16x16x32_bf16 v[200:203], a[68:71], a[36:39], v[200:203]\n"
    "s_add_u32 s55, s55, 0x20000000\n"
    "v_exp_f32 v207, v207\n"
    "v_mfma_f32_16x16x32_bf16 v[200:203], a[72:75], a[40:43], v[200:203]\n"
    "s_cselect_b32 s53, s56, s53\n"
    "v_cvt_pk_bf16_f32 v164, v192, v193\n"
    "v_mfma_f32_16x16x32_bf16 v[200:203], a[76:79], a[44:47], v[200:203]\n"
    "s_waitcnt lgkmcnt(10)\n"
    "v_exp_f32 v196, v196\n"
    "v_mfma_f32_16x16x32_bf16 v[80:83], a[136:139], v[128:131], v[80:83]\n"
    "s_waitcnt lgkmcnt(9)\n"
    "v_cvt_pk_bf16_f32 v187, v206, v207\n"
    "v_add_u32 v224, s60, v224\n"
    "v_mfma_f32_16x16x32_bf16 v[80:83], a[140:143], v[132:135], v[80:83]\n"
    "s_add_u32 s40, s28, s53\n"
    "v_exp_f32 v197, v197\n"
    "v_mfma_f32_16x16x32_bf16 v[84:87], a[136:139], v[136:139], v[84:87]\n"
    "s_addc_u32 s41, s29, 0\n"
    "v_exp_f32 v199, v199\n"
    "v_mfma_f32_16x16x32_bf16 v[84:87], a[140:143], v[140:143], v[84:87]\n"
    "s_cmp_lt_u32 s53, 268435456\n"
    "v_exp_f32 v195, v195\n"
    "v_smfmac_f32_16x16x64_bf16 a[168:171], a[176:179], v[144:151], v237\n"
    "s_cselect_b32 s42, 4096, 0\n"
    "v_cvt_pk_bf16_f32 v165, v194, v195\n"
    "v_mfma_f32_16x16x32_bf16 v[204:207], a[64:67], a[48:51], v[252:255]\n"
    "v_exp_f32 v202, v202\n"
    "v_mfma_f32_16x16x32_bf16 v[204:207], a[68:71], a[52:55], v[204:207]\n"
    "ds_read_b32 v242, v241\n"
    "v_cvt_pk_bf16_f32 v172, v196, v197\n"
    "v_mfma_f32_16x16x32_bf16 v[204:207], a[72:75], a[56:59], v[204:207]\n"
    "v_exp_f32 v203, v203\n"
    "v_mfma_f32_16x16x32_bf16 v[204:207], a[76:79], a[60:63], v[204:207]\n"
    "v_mfma_f32_16x16x32_bf16 v[88:91], a[136:139], v[144:147], v[88:91]\n"
    "v_mfma_f32_16x16x32_bf16 v[88:91], a[140:143], v[148:151], v[88:91]\n"
    "v_exp_f32 v201, v201\n"
    "v_mfma_f32_16x16x32_bf16 v[92:95], a[136:139], v[152:155], v[92:95]\n"
    "v_mfma_f32_16x16x32_bf16 v[92:95], a[140:143], v[156:159], v[92:95]\n"
    "s_waitcnt lgkmcnt(0)\n"
    "v_cmp_gt_u32 vcc, v243, v242\n"
    "v_mfma_f32_16x16x32_bf16 v[192:195], a[80:83], a[0:3], v[208:211]\n"
    "s_cbranch_vccnz LSPTe%=\n"
    "LSRTe%=:\n"
    "ds_read_b128 a[72:75], v224 offset:512\n"
    "v_mfma_f32_16x16x32_bf16 v[192:195], a[84:87], a[4:7], v[192:195]\n"
    "s_add_u32 m0, s80, 0\n"
    "v_exp_f32 v207, v207\n"
    "v_mfma_f32_16x16x32_bf16 v[192:195], a[88:91], a[8:11], v[192:195]\n"
    "ds_read_b128 a[76:79], v224 offset:768\n"
    "v_mfma_f32_16x16x32_bf16 v[192:195], a[92:95], a[12:15], v[192:195]\n"
    "buffer_load_dwordx4 v229, s[40:43], 0 offen nt lds\n"
    "v_cvt_pk_bf16_f32 v173, v198, v199\n"
    "v_mfma_f32_16x16x32_bf16 v[96:99], a[144:147], v[128:131], v[96:99]\n"
    "v_exp_f32 v200, v200\n"
    "v_mfma_f32_16x16x32_bf16 v[96:99], a[148:151], v[132:135], v[96:99]\n"
    "ds_read_b128 a[68:71], v224 offset:256\n"
    "ds_read_b64_tr_b16 a[130:131], v225 offset:20480\n"
    "v_mfma_f32_16x16x32_bf16 v[100:103], a[144:147], v[136:139], v[100:103]\n"
    "v_exp_f32 v204, v204\n"
    "v_mfma_f32_16x16x32_bf16 v[100:103], a[148:151], v[140:143], v[100:103]\n"
    "v_exp_f32 v206, v206\n"
    "v_mfma_f32_16x16x32_bf16 v[196:199], a[80:83], a[16:19], v[212:215]\n"
    "v_exp_f32 v205, v205\n"
    "v_mfma_f32_16x16x32_bf16 v[196:199], a[84:87], a[20:23], v[196:199]\n"
    "ds_read_b64_tr_b16 a[140:141], v225 offset:24832\n"
    "v_cvt_pk_bf16_f32 v189, v206, v207\n"
    "v_mfma_f32_16x16x32_bf16 v[196:199], a[88:91], a[24:27], v[196:199]\n"
    "v_exp_f32 v193, v193\n"
    "v_mfma_f32_16x16x32_bf16 v[196:199], a[92:95], a[28:31], v[196:199]\n"
    "buffer_load_dwordx4 v229, s[40:43], 0 offen offset:2048 nt lds\n"
    "ds_read_b64_tr_b16 a[142:143], v225 offset:28928\n"
    "v_mfma_f32_16x16x32_bf16 v[104:107], a[144:147], v[144:147], v[104:107]\n"
    "s_add_u32 m0, s80, 896\n"
    "v_exp_f32 v192, v192\n"
    "v_mfma_f32_16x16x32_bf16 v[104:107], a[148:151], v[148:151], v[104:107]\n"
    "v_exp_f32 v194, v194\n"
    "v_mfma_f32_16x16x32_bf16 v[108:111], a[144:147], v[152:155], v[108:111]\n"
    "ds_read_b64_tr_b16 a[136:137], v225 offset:16640\n"
    "v_cvt_pk_bf16_f32 v180, v200, v201\n"
    "v_mfma_f32_16x16x32_bf16 v[108:111], a[148:151], v[156:159], v[108:111]\n"
    "v_cvt_pk_bf16_f32 v181, v202, v203\n"
    "v_xor_b32 v223, s12, v223\n"
    "v_mfma_f32_16x16x32_bf16 v[200:203], a[80:83], a[32:35], v[248:251]\n"
    "v_exp_f32 v197, v197\n"
    "v_mfma_f32_16x16x32_bf16 v[200:203], a[84:87], a[36:39], v[200:203]\n"
    "v_exp_f32 v199, v199\n"
    "v_mfma_f32_16x16x32_bf16 v[200:203], a[88:91], a[40:43], v[200:203]\n"
    "ds_read_b64_tr_b16 a[132:133], v225 offset:24576\n"
    "v_cvt_pk_bf16_f32 v188, v204, v205\n"
    "v_mfma_f32_16x16x32_bf16 v[200:203], a[92:95], a[44:47], v[200:203]\n"
    "buffer_load_dwordx4 v229, s[40:43], 0 offen offset:128 nt lds\n"
    "ds_read_b128 a[64:67], v224 offset:0\n"
    "v_mfma_f32_16x16x32_bf16 v[112:115], a[152:155], v[128:131], v[112:115]\n"
    "v_exp_f32 v195, v195\n"
    "v_mfma_f32_16x16x32_bf16 v[112:115], a[156:159], v[132:135], v[112:115]\n"
    "v_exp_f32 v196, v196\n"
    "v_mfma_f32_16x16x32_bf16 v[116:119], a[152:155], v[136:139], v[116:119]\n"
    "ds_read_b64_tr_b16 a[128:129], v225 offset:16384\n"
    "v_cvt_pk_bf16_f32 v166, v192, v193\n"
    "v_mfma_f32_16x16x32_bf16 v[116:119], a[156:159], v[140:143], v[116:119]\n"
    "v_exp_f32 v198, v198\n"
    "v_smfmac_f32_16x16x64_bf16 a[172:175], a[176:179], v[152:159], v237\n"
    "ds_read_b64_tr_b16 a[138:139], v225 offset:20736\n"
    "v_mfma_f32_16x16x32_bf16 v[204:207], a[80:83], a[48:51], v[252:255]\n"
    "v_exp_f32 v202, v202\n"
    "v_mfma_f32_16x16x32_bf16 v[204:207], a[84:87], a[52:55], v[204:207]\n"
    "ds_read_b64_tr_b16 a[134:135], v225 offset:28672\n"
    "v_cvt_pk_bf16_f32 v167, v194, v195\n"
    "v_mfma_f32_16x16x32_bf16 v[204:207], a[88:91], a[56:59], v[204:207]\n"
    "buffer_load_dwordx4 v229, s[40:43], 0 offen offset:2176 nt lds\n"
    "v_cvt_pk_bf16_f32 v175, v198, v199\n"
    "v_mfma_f32_16x16x32_bf16 v[204:207], a[92:95], a[60:63], v[204:207]\n"
    "s_cmp_lg_u32 s70, 3\n"
    "v_exp_f32 v200, v200\n"
    "v_mfma_f32_16x16x32_bf16 v[120:123], a[152:155], v[144:147], v[120:123]\n"
    "v_exp_f32 v201, v201\n"
    "v_mfma_f32_16x16x32_bf16 v[120:123], a[156:159], v[148:151], v[120:123]\n"
    "v_cvt_pk_bf16_f32 v174, v196, v197\n"
    "v_mfma_f32_16x16x32_bf16 v[124:127], a[152:155], v[152:155], v[124:127]\n"
    "v_exp_f32 v203, v203\n"
    "v_mfma_f32_16x16x32_bf16 v[124:127], a[156:159], v[156:159], v[124:127]\n"
    "s_waitcnt lgkmcnt(3)\n"
    "v_cvt_pk_bf16_f32 v182, v200, v201\n"
    "v_cvt_pk_bf16_f32 v183, v202, v203\n"
    "s_cbranch_scc1 LTO%=\n"
    "s_cmp_eq_u32 s69, 2\n"
    "s_cbranch_scc1 LTO2%=\n"
    "v_smfmac_f32_16x16x64_bf16 a[160:163], a[176:179], v[160:167], v237\n"
    "s_waitcnt vmcnt(32)\n"
    "v_exp_f32 v205, v205\n"
    "v_smfmac_f32_16x16x64_bf16 a[164:167], a[176:179], v[168:175], v237\n"
    "s_add_u32 s53, s53, 16384\n"
    "ds_read_b128 a[0:3], v226 offset:0\n"
    "v_smfmac_f32_16x16x64_bf16 a[168:171], a[176:179], v[176:183], v237\n"
    "s_waitcnt lgkmcnt(3)\n"
    "ds_read_b128 a[12:15], v226 offset:768\n"
    "v_mfma_f32_16x16x32_bf16 v[0:3], a[128:131], v[160:163], v[0:3]\n"
    "s_waitcnt lgkmcnt(2)\n"
    "v_exp_f32 v206, v206\n"
    "v_mfma_f32_16x16x32_bf16 v[0:3], a[132:135], v[164:167], v[0:3]\n"
    "s_add_u32 s55, s55, 0x20000000\n"
    "ds_read_b128 a[8:11], v226 offset:512\n"
    "v_exp_f32 v207, v207\n"
    "v_mfma_f32_16x16x32_bf16 v[4:7], a[128:131], v[168:171], v[4:7]\n"
    "s_cselect_b32 s53, s56, s53\n"
    "v_exp_f32 v204, v204\n"
    "v_mfma_f32_16x16x32_bf16 v[4:7], a[132:135], v[172:175], v[4:7]\n"
    "s_add_u32 s40, s28, s53\n"
    "ds_read_b128 a[24:27], v226 offset:4608\n"
    "v_cvt_pk_bf16_f32 v191, v206, v207\n"
    "v_mfma_f32_16x16x32_bf16 v[8:11], a[128:131], v[176:179], v[8:11]\n"
    "s_addc_u32 s41, s29, 0\n"
    "v_cvt_pk_bf16_f32 v190, v204, v205\n"
    "v_accvgpr_read_b32 v219, a164\n"
    "v_mfma_f32_16x16x32_bf16 v[8:11], a[132:135], v[180:183], v[8:11]\n"
    "s_cmp_lt_u32 s53, 268435456\n"
    "ds_read_b128 a[28:31], v226 offset:4864\n"
    "v_accvgpr_read_b32 v217, a160\n"
    "v_accvgpr_read_b32 v216, a161\n"
    "v_accvgpr_read_b32 v218, a165\n"
    "v_add_f32 v219, v219, v218\n"
    "v_smfmac_f32_16x16x64_bf16 a[172:175], a[176:179], v[184:191], v237\n"
    "s_cselect_b32 s42, 4096, 0\n"
    "ds_read_b128 a[20:23], v226 offset:4352\n"
    "v_mfma_f32_16x16x32_bf16 v[12:15], a[128:131], v[184:187], v[12:15]\n"
    "s_add_u32 s58, s58, 16384\n"
    "ds_read_b128 a[4:7], v226 offset:256\n"
    "v_rcp_f32 v218, v219\n"
    "v_mfma_f32_16x16x32_bf16 v[12:15], a[132:135], v[188:191], v[12:15]\n"
    "s_waitcnt lgkmcnt(6)\n"
    "v_add_f32 v217, v217, v216\n"
    "v_accvgpr_read_b32 v221, a168\n"
    "v_accvgpr_read_b32 v220, a169\n"
    "v_mfma_f32_16x16x32_bf16 v[192:195], a[246:249], a[0:3], 0\n"
    "s_cmp_eq_u32 s58, 49152\n"
    "ds_read_b64_tr_b16 a[158:159], v225 offset:29440\n"
    "v_rcp_f32 v216, v217\n"
    "v_mfma_f32_16x16x32_bf16 v[192:195], a[252:255], a[0:3], v[192:195]\n"
    "s_cselect_b32 s60, s59, s72\n"
    "v_mov_b32 v217, v216\n"
    "v_add_f32 v221, v221, v220\n"
    "v_mfma_f32_16x16x32_bf16 v[16:19], a[136:139], v[160:163], v[16:19]\n"
    "s_cselect_b32 s58, 0, s58\n"
    "ds_read_b64_tr_b16 a[148:149], v225 offset:25088\n"
    "v_mov_b32 v219, v218\n"
    "v_mfma_f32_16x16x32_bf16 v[16:19], a[140:143], v[164:167], v[16:19]\n"
    "s_mov_b32 s48, s76\n"
    "ds_read_b64_tr_b16 a[144:145], v225 offset:16896\n"
    "ds_read_b64_tr_b16 a[146:147], v225 offset:20992\n"
    "v_rcp_f32 v220, v221\n"
    "v_accvgpr_read_b32 v222, a173\n"
    "v_pk_mul_f32 v[0:1], v[0:1], v[216:217]\n"
    "v_pk_mul_f32 v[6:7], v[6:7], v[218:219]\n"
    "v_mfma_f32_16x16x32_bf16 v[196:199], a[244:247], a[0:3], 0\n"
    "s_mov_b32 s49, s77\n"
    "ds_read_b128 a[16:19], v226 offset:4096\n"
    "v_mov_b32 v221, v220\n"
    "v_mfma_f32_16x16x32_bf16 v[196:199], a[250:253], a[0:3], v[196:199]\n"
    "s_mov_b32 s50, 188416\n"
    "ds_read_b64_tr_b16 a[150:151], v225 offset:29184\n"
    "v_cvt_pk_bf16_f32 v192, v192, v193\n"
    "v_mfma_f32_16x16x32_bf16 v[20:23], a[136:139], v[168:171], v[20:23]\n"
    "s_mov_b32 s74, s79\n"
    "v_accvgpr_read_b32 v223, a172\n"
    "v_add_f32 v223, v223, v222\n"
    "v_mfma_f32_16x16x32_bf16 v[20:23], a[140:143], v[172:175], v[20:23]\n"
    "s_waitcnt lgkmcnt(6)\n"
    "v_rcp_f32 v222, v223\n"
    "v_mfma_f32_16x16x32_bf16 v[200:203], a[246:249], a[4:7], 0\n"
    "s_mov_b32 s75, 0\n"
    "ds_read_b128 a[92:95], v224 offset:4864\n"
    "v_mov_b32 v223, v222\n"
    "v_mfma_f32_16x16x32_bf16 v[200:203], a[252:255], a[4:7], v[200:203]\n"
    "s_mov_b32 s76, s86\n"
    "ds_read_b64_tr_b16 a[154:155], v225 offset:21248\n"
    "v_cvt_pk_bf16_f32 v193, v194, v195\n"
    "v_cvt_pk_bf16_f32 v196, v196, v197\n"
    "v_cvt_pk_bf16_f32 v197, v198, v199\n"
    "v_accvgpr_write_b32 a3, v197\n"
    "v_pk_mul_f32 v[2:3], v[2:3], v[216:217]\n"
    "v_pk_mul_f32 v[4:5], v[4:5], v[218:219]\n"
    "v_pk_mul_f32 v[8:9], v[8:9], v[220:221]\n"
    "v_pk_mul_f32 v[10:11], v[10:11], v[220:221]\n"
    "v_pk_mul_f32 v[12:13], v[12:13], v[222:223]\n"
    "v_pk_mul_f32 v[16:17], v[16:17], v[216:217]\n"
    "v_pk_mul_f32 v[18:19], v[18:19], v[216:217]\n"
    "v_mfma_f32_16x16x32_bf16 v[24:27], a[136:139], v[176:179], v[24:27]\n"
    "s_mov_b32 s77, s87\n"
    "v_accvgpr_write_b32 a1, v193\n"
    "v_accvgpr_write_b32 a164, 0\n"
    "v_mfma_f32_16x16x32_bf16 v[24:27], a[140:143], v[180:183], v[24:27]\n"
    "s_mov_b32 s79, s88\n"
    "ds_read_b64_tr_b16 a[152:153], v225 offset:17152\n"
    "ds_read_b64_tr_b16 a[156:157], v225 offset:25344\n"
    "v_accvgpr_write_b32 a2, v196\n"
    "v_mfma_f32_16x16x32_bf16 v[204:207], a[244:247], a[4:7], 0\n"
    "s_mov_b32 s86, s89\n"
    "v_cmp_nle_f32 vcc, s63, v216\n"
    "v_accvgpr_write_b32 a165, 0\n"
    "v_mfma_f32_16x16x32_bf16 v[204:207], a[250:253], a[4:7], v[204:207]\n"
    "s_mov_b32 s87, s90\n"
    "s_or_b64 s[66:67], s[66:67], vcc\n"
    "v_accvgpr_write_b32 a0, v192\n"
    "v_cvt_pk_bf16_f32 v200, v200, v201\n"
    "v_mfma_f32_16x16x32_bf16 v[28:31], a[136:139], v[184:187], v[28:31]\n"
    "s_mov_b32 s88, s91\n"
    "ds_read_b128 a[88:91], v224 offset:4608\n"
    "v_cvt_pk_bf16_f32 v16, v16, v17\n"
    "v_mfma_f32_16x16x32_bf16 v[28:31], a[140:143], v[188:191], v[28:31]\n"
    "s_mov_b32 s44, s82\n"
    "v_cvt_pk_bf16_f32 v201, v202, v203\n"
    "v_cmp_nle_f32 vcc, s63, v218\n"
    "v_mfma_f32_16x16x32_bf16 v[192:195], a[246:249], a[8:11], 0\n"
    "s_mov_b32 s45, s83\n"
    "s_or_b64 s[66:67], s[66:67], vcc\n"
    "ds_read_b64_tr_b16 a[132:133], v225 offset:25600\n"
    "ds_read_b128 a[84:87], v224 offset:4352\n"
    "v_mfma_f32_16x16x32_bf16 v[192:195], a[252:255], a[8:11], v[192:195]\n"
    "s_waitcnt lgkmcnt(9)\n"
    "ds_read_b64_tr_b16 a[128:129], v225 offset:17408\n"
    "ds_read_b128 a[120:123], v224 offset:12800\n"
    "v_mfma_f32_16x16x32_bf16 v[32:35], a[144:147], v[160:163], v[32:35]\n"
    "s_waitcnt lgkmcnt(9)\n"
    "v_accvgpr_write_b32 a4, v200\n"
    "v_cmp_nle_f32 vcc, s63, v220\n"
    "v_accvgpr_write_b32 a172, 0\n"
    "v_cvt_pk_bf16_f32 v4, v4, v5\n"
    "v_cvt_pk_bf16_f32 v12, v12, v13\n"
    "v_pk_mul_f32 v[20:21], v[20:21], v[218:219]\n"
    "v_pk_mul_f32 v[22:23], v[22:23], v[218:219]\n"
    "v_pk_mul_f32 v[24:25], v[24:25], v[220:221]\n"
    "v_mfma_f32_16x16x32_bf16 v[32:35], a[148:151], v[164:167], v[32:35]\n"
    "s_mov_b32 m0, s57\n"
    "s_or_b64 s[66:67], s[66:67], vcc\n"
    "ds_read_b64_tr_b16 a[136:137], v225 offset:17664\n"
    "ds_read_b64_tr_b16 a[138:139], v225 offset:21760\n"
    "v_mfma_f32_16x16x32_bf16 v[196:199], a[244:247], a[8:11], 0\n"
    "s_mov_b32 s46, s84\n"
    "v_cvt_pk_bf16_f32 v204, v204, v205\n"
    "v_cvt_pk_bf16_f32 v205, v206, v207\n"
    "v_mfma_f32_16x16x32_bf16 v[196:199], a[250:253], a[8:11], v[196:199]\n"
    "s_mov_b32 s8, 3578789888\n"
    "ds_read_b64_tr_b16 a[134:135], v225 offset:29696\n"
    "v_accvgpr_write_b32 a5, v201\n"
    "v_cmp_nle_f32 vcc, s63, v222\n"
    "v_mfma_f32_16x16x32_bf16 v[36:39], a[144:147], v[168:171], v[36:39]\n"
    "s_mov_b32 s82, s18\n"
    "s_or_b64 s[66:67], s[66:67], vcc\n"
    "ds_read_b128 a[108:111], v224 offset:8960\n"
    "buffer_load_dwordx4 v227, s[36:39], 0 offen nt lds\n"
    "v_mfma_f32_16x16x32_bf16 v[36:39], a[148:151], v[172:175], v[36:39]\n"
    "s_mov_b32 s83, s19\n"
    "v_add_u32 v234, 4294950912, v234\n"
    "v_cvt_pk_bf16_f32 v8, v8, v9\n"
    "v_mfma_f32_16x16x32_bf16 v[200:203], a[246:249], a[12:15], 0\n"
    "s_mov_b32 s84, 8192\n"
    "ds_read_b64_tr_b16 a[140:141], v225 offset:25856\n"
    "ds_read_b128 a[96:99], v224 offset:8192\n"
    "v_accvgpr_write_b32 a6, v204\n"
    "v_cvt_pk_bf16_f32 v192, v192, v193\n"
    "v_cvt_pk_bf16_f32 v196, v196, v197\n"
    "v_cvt_pk_bf16_f32 v17, v18, v19\n"
    "v_pk_mul_f32 v[14:15], v[14:15], v[222:223]\n"
    "v_pk_mul_f32 v[26:27], v[26:27], v[220:221]\n"
    "v_pk_mul_f32 v[28:29], v[28:29], v[222:223]\n"
    "v_pk_mul_f32 v[30:31], v[30:31], v[222:223]\n"
    "v_pk_mul_f32 v[32:33], v[32:33], v[216:217]\n"
    "v_pk_mul_f32 v[34:35], v[34:35], v[216:217]\n"
    "v_mfma_f32_16x16x32_bf16 v[200:203], a[252:255], a[12:15], v[200:203]\n"
    "s_mov_b32 s61, s85\n"
    "v_cvt_pk_bf16_f32 v0, v0, v1\n"
    "v_cvt_pk_bf16_f32 v20, v20, v21\n"
    "v_mfma_f32_16x16x32_bf16 v[40:43], a[144:147], v[176:179], v[40:43]\n"
    "s_mov_b32 s85, s99\n"
    "v_accvgpr_write_b32 a10, v196\n"
    "v_cvt_pk_bf16_f32 v5, v6, v7\n"
    "v_mfma_f32_16x16x32_bf16 v[40:43], a[148:151], v[180:183], v[40:43]\n"
    "s_mov_b32 s73, s62\n"
    "v_accvgpr_write_b32 a7, v205\n"
    "v_accvgpr_write_b32 a8, v192\n"
    "v_cvt_pk_bf16_f32 v193, v194, v195\n"
    "v_cvt_pk_bf16_f32 v24, v24, v25\n"
    "v_mfma_f32_16x16x32_bf16 v[204:207], a[244:247], a[12:15], 0\n"
    "s_mov_b32 s56, s78\n"
    "ds_read_b64_tr_b16 a[142:143], v225 offset:29952\n"
    "v_cvt_pk_bf16_f32 v197, v198, v199\n"
    "v_mfma_f32_16x16x32_bf16 v[204:207], a[250:253], a[12:15], v[204:207]\n"
    "s_mov_b32 s78, 268435456\n"
    "v_accvgpr_write_b32 a9, v193\n"
    "v_accvgpr_write_b32 a168, 0\n"
    "v_mfma_f32_16x16x32_bf16 v[44:47], a[144:147], v[184:187], v[44:47]\n"
    "v_cvt_pk_bf16_f32 v25, v26, v27\n"
    "v_cvt_pk_bf16_f32 v32, v32, v33\n"
    "v_mfma_f32_16x16x32_bf16 v[44:47], a[148:151], v[188:191], v[44:47]\n"
    "ds_write_b64 v231, v[16:17] offset:0\n"
    "v_mfma_f32_16x16x32_bf16 v[192:195], a[246:249], a[16:19], 0\n"
    "ds_read_b64_tr_b16 a[130:131], v225 offset:21504\n"
    "v_cvt_pk_bf16_f32 v200, v200, v201\n"
    "v_cvt_pk_bf16_f32 v21, v22, v23\n"
    "v_cvt_pk_bf16_f32 v33, v34, v35\n"
    "v_mfma_f32_16x16x32_bf16 v[192:195], a[252:255], a[16:19], v[192:195]\n"
    "s_waitcnt lgkmcnt(15)\n"
    "ds_read_b128 a[104:107], v224 offset:8704\n"
    "v_cvt_pk_bf16_f32 v28, v28, v29\n"
    "v_mfma_f32_16x16x32_bf16 v[48:51], a[152:155], v[160:163], v[48:51]\n"
    "s_waitcnt lgkmcnt(15)\n"
    "ds_read_b128 a[80:83], v224 offset:4096\n"
    "buffer_load_dwordx4 v227, s[36:39], 0 offen offset:1024 nt lds\n"
    "v_accvgpr_write_b32 a11, v197\n"
    "v_cvt_pk_bf16_f32 v201, v202, v203\n"
    "v_cvt_pk_bf16_f32 v13, v14, v15\n"
    "v_pk_mul_f32 v[40:41], v[40:41], v[220:221]\n"
    "v_mfma_f32_16x16x32_bf16 v[48:51], a[156:159], v[164:167], v[48:51]\n"
    "ds_read_b128 a[112:115], v224 offset:12288\n"
    "ds_write_b64 v231, v[24:25] offset:4096\n"
    "v_accvgpr_write_b32 a160, 0\n"
    "v_pk_mul_f32 v[36:37], v[36:37], v[218:219]\n"
    "v_mfma_f32_16x16x32_bf16 v[196:199], a[244:247], a[16:19], 0\n"
    "s_waitcnt vmcnt(6)\n"
    "ds_read_b64_tr_b16 a[144:145], v225 offset:17920\n"
    "v_accvgpr_write_b32 a13, v201\n"
    "v_cvt_pk_bf16_f32 v192, v192, v193\n"
    "v_mfma_f32_16x16x32_bf16 v[196:199], a[250:253], a[16:19], v[196:199]\n"
    "ds_read_b128 a[100:103], v224 offset:8448\n"
    "v_cvt_pk_bf16_f32 v204, v204, v205\n"
    "v_cvt_pk_bf16_f32 v29, v30, v31\n"
    "v_mfma_f32_16x16x32_bf16 v[52:55], a[152:155], v[168:171], v[52:55]\n"
    "ds_read_b128 a[40:43], v226 offset:8704\n"
    "v_cvt_pk_bf16_f32 v205, v206, v207\n"
    "v_mfma_f32_16x16x32_bf16 v[52:55], a[156:159], v[172:175], v[52:55]\n"
    "ds_read_b128 a[52:55], v226 offset:12544\n"
    "ds_read_b128 a[56:59], v226 offset:12800\n"
    "v_accvgpr_write_b32 a12, v200\n"
    "v_mfma_f32_16x16x32_bf16 v[200:203], a[246:249], a[20:23], 0\n"
    "ds_read_b64_tr_b16 a[146:147], v225 offset:22016\n"
    "v_accvgpr_write_b32 a15, v205\n"
    "v_mfma_f32_16x16x32_bf16 v[200:203], a[252:255], a[20:23], v[200:203]\n"
    "v_accvgpr_write_b32 a14, v204\n"
    "v_cvt_pk_bf16_f32 v196, v196, v197\n"
    "v_mfma_f32_16x16x32_bf16 v[56:59], a[152:155], v[176:179], v[56:59]\n"
    "ds_read_b128 a[36:39], v226 offset:8448\n"
    "v_cvt_pk_bf16_f32 v193, v194, v195\n"
    "v_mfma_f32_16x16x32_bf16 v[56:59], a[156:159], v[180:183], v[56:59]\n"
    "ds_read_b128 a[48:51], v226 offset:12288\n"
    "ds_read_b128 a[60:63], v226 offset:13056\n"
    "v_accvgpr_write_b32 a18, v196\n"
    "v_mfma_f32_16x16x32_bf16 v[204:207], a[244:247], a[20:23], 0\n"
    "ds_read_b128 a[44:47], v226 offset:8960\n"
    "ds_write_b64 v230, v[12:13] offset:6144\n"
    "v_accvgpr_write_b32 a16, v192\n"
    "v_pk_mul_f32 v[48:49], v[48:49], v[216:217]\n"
    "v_mfma_f32_16x16x32_bf16 v[204:207], a[250:253], a[20:23], v[204:207]\n"
    "ds_read_b128 a[32:35], v226 offset:8192\n"
    "v_cvt_pk_bf16_f32 v197, v198, v199\n"
    "v_accvgpr_write_b32 a19, v197\n"
    "v_cvt_pk_bf16_f32 v9, v10, v11\n"
    "v_mfma_f32_16x16x32_bf16 v[60:63], a[152:155], v[184:187], v[60:63]\n"
    "buffer_load_dwordx4 v228, s[36:39], 0 offen offset:2048 nt lds\n"
    "v_cvt_pk_bf16_f32 v200, v200, v201\n"
    "v_mfma_f32_16x16x32_bf16 v[60:63], a[156:159], v[188:191], v[60:63]\n"
    "ds_write_b64 v230, v[4:5] offset:2048\n"
    "v_accvgpr_write_b32 a17, v193\n"
    "v_mfma_f32_16x16x32_bf16 v[192:195], a[246:249], a[24:27], 0\n"
    "v_cvt_pk_bf16_f32 v201, v202, v203\n"
    "v_cvt_pk_bf16_f32 v36, v36, v37\n"
    "v_mfma_f32_16x16x32_bf16 v[192:195], a[252:255], a[24:27], v[192:195]\n"
    "s_waitcnt lgkmcnt(15)\n"
    "ds_write_b64 v230, v[8:9] offset:4096\n"
    "v_accvgpr_write_b32 a169, 0\n"
    "v_cvt_pk_bf16_f32 v48, v48, v49\n"
    "v_pk_mul_f32 v[38:39], v[38:39], v[218:219]\n"
    "v_pk_mul_f32 v[42:43], v[42:43], v[220:221]\n"
    "v_pk_mul_f32 v[46:47], v[46:47], v[222:223]\n"
    "v_pk_mul_f32 v[52:53], v[52:53], v[218:219]\n"
    "v_pk_mul_f32 v[54:55], v[54:55], v[218:219]\n"
    "v_pk_mul_f32 v[56:57], v[56:57], v[220:221]\n"
    "v_mfma_f32_16x16x32_bf16 v[64:67], a[128:131], v[160:163], v[64:67]\n"
    "ds_read_b64_tr_b16 a[154:155], v225 offset:22272\n"
    "v_accvgpr_write_b32 a20, v200\n"
    "v_mfma_f32_16x16x32_bf16 v[64:67], a[132:135], v[164:167], v[64:67]\n"
    "ds_read_b128 a[124:127], v224 offset:13056\n"
    "v_cvt_pk_bf16_f32 v1, v2, v3\n"
    "v_mfma_f32_16x16x32_bf16 v[196:199], a[244:247], a[24:27], 0\n"
    "ds_read_b64_tr_b16 a[148:149], v225 offset:26112\n"
    "v_cvt_pk_bf16_f32 v204, v204, v205\n"
    "v_mfma_f32_16x16x32_bf16 v[196:199], a[250:253], a[24:27], v[196:199]\n"
    "ds_write_b64 v230, v[0:1] offset:0\n"
    "v_mfma_f32_16x16x32_bf16 v[68:71], a[128:131], v[168:171], v[68:71]\n"
    "ds_read_b64_tr_b16 a[150:151], v225 offset:30208\n"
    "v_cvt_pk_bf16_f32 v205, v206, v207\n"
    "v_mfma_f32_16x16x32_bf16 v[68:71], a[132:135], v[172:175], v[68:71]\n"
    "v_accvgpr_write_b32 a21, v201\n"
    "v_accvgpr_write_b32 a23, v205\n"
    "v_mfma_f32_16x16x32_bf16 v[200:203], a[246:249], a[28:31], 0\n"
    "v_cvt_pk_bf16_f32 v192, v192, v193\n"
    "v_cvt_pk_bf16_f32 v196, v196, v197\n"
    "v_mfma_f32_16x16x32_bf16 v[200:203], a[252:255], a[28:31], v[200:203]\n"
    "ds_read_b128 a[116:119], v224 offset:12544\n"
    "v_accvgpr_write_b32 a22, v204\n"
    "v_accvgpr_write_b32 a26, v196\n"
    "v_cvt_pk_bf16_f32 v40, v40, v41\n"
    "v_cvt_pk_bf16_f32 v52, v52, v53\n"
    "v_pk_mul_f32 v[44:45], v[44:45], v[222:223]\n"
    "v_pk_mul_f32 v[50:51], v[50:51], v[216:217]\n"
    "v_pk_mul_f32 v[58:59], v[58:59], v[220:221]\n"
    "v_pk_mul_f32 v[60:61], v[60:61], v[222:223]\n"
    "v_pk_mul_f32 v[64:65], v[64:65], v[216:217]\n"
    "v_mfma_f32_16x16x32_bf16 v[72:75], a[128:131], v[176:179], v[72:75]\n"
    "buffer_load_dwordx4 v228, s[36:39], 0 offen offset:3072 nt lds\n"
    "v_cvt_pk_bf16_f32 v193, v194, v195\n"
    "v_mfma_f32_16x16x32_bf16 v[72:75], a[132:135], v[180:183], v[72:75]\n"
    "s_add_u32 s52, s52, 16384\n"
    "v_accvgpr_write_b32 a24, v192\n"
    "v_cvt_pk_bf16_f32 v197, v198, v199\n"
    "v_mfma_f32_16x16x32_bf16 v[204:207], a[244:247], a[28:31], 0\n"
    "s_add_u32 s54, s54, 0x20000000\n"
    "v_accvgpr_write_b32 a161, 0\n"
    "v_cvt_pk_bf16_f32 v60, v60, v61\n"
    "v_mfma_f32_16x16x32_bf16 v[204:207], a[250:253], a[28:31], v[204:207]\n"
    "s_cselect_b32 s52, s56, s52\n"
    "v_accvgpr_write_b32 a25, v193\n"
    "v_cvt_pk_bf16_f32 v37, v38, v39\n"
    "v_mfma_f32_16x16x32_bf16 v[76:79], a[128:131], v[184:187], v[76:79]\n"
    "s_add_u32 s36, s26, s52\n"
    "v_cvt_pk_bf16_f32 v200, v200, v201\n"
    "v_accvgpr_write_b32 a28, v200\n"
    "v_mfma_f32_16x16x32_bf16 v[76:79], a[132:135], v[188:191], v[76:79]\n"
    "s_waitcnt lgkmcnt(8)\n"
    "v_accvgpr_write_b32 a27, v197\n"
    "v_cvt_pk_bf16_f32 v53, v54, v55\n"
    "v_mfma_f32_16x16x32_bf16 v[192:195], a[246:249], a[32:35], 0\n"
    "s_addc_u32 s37, s27, 0\n"
    "v_cvt_pk_bf16_f32 v204, v204, v205\n"
    "v_cvt_pk_bf16_f32 v56, v56, v57\n"
    "v_mfma_f32_16x16x32_bf16 v[192:195], a[252:255], a[32:35], v[192:195]\n"
    "s_cmp_lt_u32 s52, 268435456\n"
    "v_accvgpr_write_b32 a173, 0\n"
    "v_cvt_pk_bf16_f32 v44, v44, v45\n"
    "v_mfma_f32_16x16x32_bf16 v[80:83], a[136:139], v[160:163], v[80:83]\n"
    "s_cselect_b32 s38, 4096, 0\n"
    "ds_read_b64_tr_b16 a[158:159], v225 offset:30464\n"
    "ds_write_b64 v231, v[20:21] offset:2048\n"
    "v_pk_mul_f32 v[62:63], v[62:63], v[222:223]\n"
    "v_pk_mul_f32 v[66:67], v[66:67], v[216:217]\n"
    "v_pk_mul_f32 v[68:69], v[68:69], v[218:219]\n"
    "v_mfma_f32_16x16x32_bf16 v[80:83], a[140:143], v[164:167], v[80:83]\n"
    "s_add_u32 s57, s57, 16384\n"
    "v_cvt_pk_bf16_f32 v201, v202, v203\n"
    "v_accvgpr_write_b32 a30, v204\n"
    "v_mfma_f32_16x16x32_bf16 v[196:199], a[244:247], a[32:35], 0\n"
    "s_cmp_ge_u32 s57, s92\n"
    "ds_read_b64_tr_b16 a[152:153], v225 offset:18176\n"
    "v_cvt_pk_bf16_f32 v64, v64, v65\n"
    "v_mfma_f32_16x16x32_bf16 v[196:199], a[250:253], a[32:35], v[196:199]\n"
    "s_cselect_b32 s71, 49152, 0\n"
    "v_cvt_pk_bf16_f32 v192, v192, v193\n"
    "v_cvt_pk_bf16_f32 v57, v58, v59\n"
    "v_mfma_f32_16x16x32_bf16 v[84:87], a[136:139], v[168:171], v[84:87]\n"
    "s_sub_u32 s57, s57, s71\n"
    "v_accvgpr_write_b32 a29, v201\n"
    "v_cvt_pk_bf16_f32 v68, v68, v69\n"
    "v_mfma_f32_16x16x32_bf16 v[84:87], a[140:143], v[172:175], v[84:87]\n"
    "s_add_u32 s55, s55, s17\n"
    "v_accvgpr_write_b32 a32, v192\n"
    "v_cvt_pk_bf16_f32 v61, v62, v63\n"
    "v_mfma_f32_16x16x32_bf16 v[200:203], a[246:249], a[36:39], 0\n"
    "s_cmp_eq_u32 s17, 0\n"
    "ds_write_b64 v233, v[60:61] offset:6144\n"
    "v_mfma_f32_16x16x32_bf16 v[200:203], a[252:255], a[36:39], v[200:203]\n"
    "s_cselect_b32 s20, 2048, s17\n"
    "v_cvt_pk_bf16_f32 v196, v196, v197\n"
    "v_cvt_pk_bf16_f32 v41, v42, v43\n"
    "v_mfma_f32_16x16x32_bf16 v[88:91], a[136:139], v[176:179], v[88:91]\n"
    "s_min_u32 s20, s20, 14336\n"
    "ds_write_b64 v232, v[32:33] offset:0\n"
    "v_cvt_pk_bf16_f32 v205, v206, v207\n"
    "v_accvgpr_write_b32 a31, v205\n"
    "v_pk_mul_f32 v[70:71], v[70:71], v[218:219]\n"
    "v_pk_mul_f32 v[72:73], v[72:73], v[220:221]\n"
    "v_pk_mul_f32 v[74:75], v[74:75], v[220:221]\n"
    "v_pk_mul_f32 v[76:77], v[76:77], v[222:223]\n"
    "v_pk_mul_f32 v[78:79], v[78:79], v[222:223]\n"
    "v_pk_mul_f32 v[80:81], v[80:81], v[216:217]\n"
    "v_mfma_f32_16x16x32_bf16 v[88:91], a[140:143], v[180:183], v[88:91]\n"
    "s_mov_b32 s17, 1073741824\n"
    "ds_write_b64 v231, v[28:29] offset:6144\n"
    "v_mfma_f32_16x16x32_bf16 v[204:207], a[244:247], a[36:39], 0\n"
    "v_cvt_pk_bf16_f32 v200, v200, v201\n"
    "v_cvt_pk_bf16_f32 v65, v66, v67\n"
    "v_mfma_f32_16x16x32_bf16 v[204:207], a[250:253], a[36:39], v[204:207]\n"
    "v_accvgpr_write_b32 a34, v196\n"
    "v_cvt_pk_bf16_f32 v45, v46, v47\n"
    "v_mfma_f32_16x16x32_bf16 v[92:95], a[136:139], v[184:187], v[92:95]\n"
    "v_cvt_pk_bf16_f32 v193, v194, v195\n"
    "v_accvgpr_write_b32 a33, v193\n"
    "v_mfma_f32_16x16x32_bf16 v[92:95], a[140:143], v[188:191], v[92:95]\n"
    "ds_write_b64 v230, v[64:65] offset:8192\n"
    "v_mfma_f32_16x16x32_bf16 v[192:195], a[246:249], a[40:43], 0\n"
    "v_cvt_pk_bf16_f32 v197, v198, v199\n"
    "v_cvt_pk_bf16_f32 v204, v204, v205\n"
    "v_mfma_f32_16x16x32_bf16 v[192:195], a[252:255], a[40:43], v[192:195]\n"
    "ds_read_b64_tr_b16 a[156:157], v225 offset:26368\n"
    "ds_write_b64 v233, v[56:57] offset:4096\n"
    "v_cvt_pk_bf16_f32 v49, v50, v51\n"
    "v_pk_mul_f32 v[82:83], v[82:83], v[216:217]\n"
    "v_pk_mul_f32 v[84:85], v[84:85], v[218:219]\n"
    "v_pk_mul_f32 v[88:89], v[88:89], v[220:221]\n"
    "v_pk_mul_f32 v[90:91], v[90:91], v[220:221]\n"
    "v_mfma_f32_16x16x32_bf16 v[96:99], a[144:147], v[160:163], v[96:99]\n"
    "s_waitcnt lgkmcnt(10)\n"
    "v_accvgpr_write_b32 a35, v197\n"
    "v_accvgpr_write_b32 a36, v200\n"
    "v_mfma_f32_16x16x32_bf16 v[96:99], a[148:151], v[164:167], v[96:99]\n"
    "s_cmp_lg_u64 s[66:67], 0\n"
    "v_cvt_pk_bf16_f32 v205, v206, v207\n"
    "v_cvt_pk_bf16_f32 v72, v72, v73\n"
    "v_mfma_f32_16x16x32_bf16 v[196:199], a[244:247], a[40:43], 0\n"
    "s_addc_u32 s68, s68, s68\n"
    "v_cvt_pk_bf16_f32 v201, v202, v203\n"
    "v_accvgpr_write_b32 a38, v204\n"
    "v_mfma_f32_16x16x32_bf16 v[196:199], a[250:253], a[40:43], v[196:199]\n"
    "s_mov_b64 s[66:67], 0\n"
    "v_cvt_pk_bf16_f32 v69, v70, v71\n"
    "v_cvt_pk_bf16_f32 v76, v76, v77\n"
    "v_mfma_f32_16x16x32_bf16 v[100:103], a[144:147], v[168:171], v[100:103]\n"
    "s_add_u32 s69, s69, 1\n"
    "ds_write_b64 v232, v[44:45] offset:6144\n"
    "v_cvt_pk_bf16_f32 v80, v80, v81\n"
    "v_mfma_f32_16x16x32_bf16 v[100:103], a[148:151], v[172:175], v[100:103]\n"
    "v_accvgpr_write_b32 a37, v201\n"
    "v_cvt_pk_bf16_f32 v73, v74, v75\n"
    "v_mfma_f32_16x16x32_bf16 v[200:203], a[246:249], a[44:47], 0\n"
    "ds_write_b64 v232, v[36:37] offset:2048\n"
    "v_cvt_pk_bf16_f32 v81, v82, v83\n"
    "v_mfma_f32_16x16x32_bf16 v[200:203], a[252:255], a[44:47], v[200:203]\n"
    "ds_write_b64 v232, v[40:41] offset:4096\n"
    "v_cvt_pk_bf16_f32 v196, v196, v197\n"
    "v_mfma_f32_16x16x32_bf16 v[104:107], a[144:147], v[176:179], v[104:107]\n"
    "ds_write_b64 v233, v[48:49] offset:0\n"
    "v_accvgpr_write_b32 a39, v205\n"
    "v_cvt_pk_bf16_f32 v192, v192, v193\n"
    "v_pk_mul_f32 v[86:87], v[86:87], v[218:219]\n"
    "v_pk_mul_f32 v[92:93], v[92:93], v[222:223]\n"
    "v_pk_mul_f32 v[94:95], v[94:95], v[222:223]\n"
    "v_mfma_f32_16x16x32_bf16 v[104:107], a[148:151], v[180:183], v[104:107]\n"
    "v_cvt_pk_bf16_f32 v193, v194, v195\n"
    "v_cvt_pk_bf16_f32 v88, v88, v89\n"
    "v_mfma_f32_16x16x32_bf16 v[204:207], a[244:247], a[44:47], 0\n"
    "v_accvgpr_write_b32 a40, v192\n"
    "v_cvt_pk_bf16_f32 v84, v84, v85\n"
    "v_mfma_f32_16x16x32_bf16 v[204:207], a[250:253], a[44:47], v[204:207]\n"
    "ds_write_b64 v230, v[68:69] offset:10240\n"
    "v_accvgpr_write_b32 a41, v193\n"
    "v_cvt_pk_bf16_f32 v85, v86, v87\n"
    "v_pk_mul_f32 v[96:97], v[96:97], v[216:217]\n"
    "v_pk_mul_f32 v[100:101], v[100:101], v[218:219]\n"
    "v_mfma_f32_16x16x32_bf16 v[108:111], a[144:147], v[184:187], v[108:111]\n"
    "v_accvgpr_write_b32 a42, v196\n"
    "v_cvt_pk_bf16_f32 v197, v198, v199\n"
    "v_mfma_f32_16x16x32_bf16 v[108:111], a[148:151], v[188:191], v[108:111]\n"
    "ds_write_b64 v230, v[72:73] offset:12288\n"
    "v_mfma_f32_16x16x32_bf16 v[192:195], a[246:249], a[48:51], 0\n"
    "v_cvt_pk_bf16_f32 v204, v204, v205\n"
    "v_cvt_pk_bf16_f32 v100, v100, v101\n"
    "v_mfma_f32_16x16x32_bf16 v[192:195], a[252:255], a[48:51], v[192:195]\n"
    "s_waitcnt lgkmcnt(12)\n"
    "v_accvgpr_write_b32 a43, v197\n"
    "v_accvgpr_write_b32 a46, v204\n"
    "v_mfma_f32_16x16x32_bf16 v[112:115], a[152:155], v[160:163], v[112:115]\n"
    "s_waitcnt lgkmcnt(7)\n"
    "v_cvt_pk_bf16_f32 v200, v200, v201\n"
    "v_cvt_pk_bf16_f32 v92, v92, v93\n"
    "v_mfma_f32_16x16x32_bf16 v[112:115], a[156:159], v[164:167], v[112:115]\n"
    "ds_write_b64 v231, v[84:85] offset:10240\n"
    "v_cvt_pk_bf16_f32 v201, v202, v203\n"
    "v_mfma_f32_16x16x32_bf16 v[196:199], a[244:247], a[48:51], 0\n"
    "v_cvt_pk_bf16_f32 v205, v206, v207\n"
    "v_cvt_pk_bf16_f32 v89, v90, v91\n"
    "v_mfma_f32_16x16x32_bf16 v[196:199], a[250:253], a[48:51], v[196:199]\n"
    "v_accvgpr_write_b32 a45, v201\n"
    "v_cvt_pk_bf16_f32 v93, v94, v95\n"
    "v_mfma_f32_16x16x32_bf16 v[116:119], a[152:155], v[168:171], v[116:119]\n"
    "ds_write_b64 v233, v[52:53] offset:2048\n"
    "v_cvt_pk_bf16_f32 v192, v192, v193\n"
    "v_cvt_pk_bf16_f32 v193, v194, v195\n"
    "v_pk_mul_f32 v[98:99], v[98:99], v[216:217]\n"
    "v_pk_mul_f32 v[108:109], v[108:109], v[222:223]\n"
    "v_mfma_f32_16x16x32_bf16 v[116:119], a[156:159], v[172:175], v[116:119]\n"
    "v_accvgpr_write_b32 a44, v200\n"
    "v_accvgpr_write_b32 a47, v205\n"
    "v_mfma_f32_16x16x32_bf16 v[200:203], a[246:249], a[52:55], 0\n"
    "ds_write_b64 v231, v[88:89] offset:12288\n"
    "v_accvgpr_write_b32 a49, v193\n"
    "v_cvt_pk_bf16_f32 v96, v96, v97\n"
    "v_pk_mul_f32 v[102:103], v[102:103], v[218:219]\n"
    "v_pk_mul_f32 v[104:105], v[104:105], v[220:221]\n"
    "v_pk_mul_f32 v[106:107], v[106:107], v[220:221]\n"
    "v_pk_mul_f32 v[110:111], v[110:111], v[222:223]\n"
    "v_pk_mul_f32 v[112:113], v[112:113], v[216:217]\n"
    "v_mfma_f32_16x16x32_bf16 v[200:203], a[252:255], a[52:55], v[200:203]\n"
    "v_cvt_pk_bf16_f32 v77, v78, v79\n"
    "v_cvt_pk_bf16_f32 v108, v108, v109\n"
    "v_mfma_f32_16x16x32_bf16 v[120:123], a[152:155], v[176:179], v[120:123]\n"
    "ds_write_b64 v231, v[92:93] offset:14336\n"
    "v_cvt_pk_bf16_f32 v196, v196, v197\n"
    "v_mfma_f32_16x16x32_bf16 v[120:123], a[156:159], v[180:183], v[120:123]\n"
    "v_cvt_pk_bf16_f32 v101, v102, v103\n"
    "v_cvt_pk_bf16_f32 v112, v112, v113\n"
    "v_mfma_f32_16x16x32_bf16 v[204:207], a[244:247], a[52:55], 0\n"
    "ds_write_b64 v231, v[80:81] offset:8192\n"
    "v_accvgpr_write_b32 a50, v196\n"
    "v_mfma_f32_16x16x32_bf16 v[204:207], a[250:253], a[52:55], v[204:207]\n"
    "v_cvt_pk_bf16_f32 v197, v198, v199\n"
    "v_cvt_pk_bf16_f32 v97, v98, v99\n"
    "v_mfma_f32_16x16x32_bf16 v[124:127], a[152:155], v[184:187], v[124:127]\n"
    "ds_write_b64 v232, v[96:97] offset:8192\n"
    "v_accvgpr_write_b32 a48, v192\n"
    "v_accvgpr_write_b32 a51, v197\n"
    "v_pk_mul_f32 v[114:115], v[114:115], v[216:217]\n"
    "v_mfma_f32_16x16x32_bf16 v[124:127], a[156:159], v[188:191], v[124:127]\n"
    "v_cvt_pk_bf16_f32 v200, v200, v201\n"
    "v_accvgpr_write_b32 a52, v200\n"
    "v_mfma_f32_16x16x32_bf16 v[192:195], a[246:249], a[56:59], 0\n"
    "v_cvt_pk_bf16_f32 v204, v204, v205\n"
    "v_cvt_pk_bf16_f32 v205, v206, v207\n"
    "v_mfma_f32_16x16x32_bf16 v[192:195], a[252:255], a[56:59], v[192:195]\n"
    "v_cvt_pk_bf16_f32 v201, v202, v203\n"
    "v_accvgpr_write_b32 a54, v204\n"
    "v_cvt_pk_bf16_f32 v113, v114, v115\n"
    "v_mfma_f32_16x16x32_bf16 v[196:199], a[244:247], a[56:59], 0\n"
    "v_cvt_pk_bf16_f32 v104, v104, v105\n"
    "v_cvt_pk_bf16_f32 v105, v106, v107\n"
    "v_mfma_f32_16x16x32_bf16 v[196:199], a[250:253], a[56:59], v[196:199]\n"
    "ds_write_b64 v230, v[76:77] offset:14336\n"
    "ds_write_b64 v232, v[100:101] offset:10240\n"
    "v_accvgpr_write_b32 a53, v201\n"
    "v_mfma_f32_16x16x32_bf16 v[200:203], a[246:249], a[60:63], 0\n"
    "ds_write_b64 v233, v[112:113] offset:8192\n"
    "v_accvgpr_write_b32 a55, v205\n"
    "v_cvt_pk_bf16_f32 v192, v192, v193\n"
    "v_accvgpr_write_b32 a56, v192\n"
    "v_cvt_pk_bf16_f32 v193, v194, v195\n"
    "v_mfma_f32_16x16x32_bf16 v[200:203], a[252:255], a[60:63], v[200:203]\n"
    "ds_write_b64 v232, v[104:105] offset:12288\n"
    "v_cvt_pk_bf16_f32 v109, v110, v111\n"
    "v_mfma_f32_16x16x32_bf16 v[204:207], a[244:247], a[60:63], 0\n"
    "ds_write_b64 v232, v[108:109] offset:14336\n"
    "v_accvgpr_write_b32 a57, v193\n"
    "v_mfma_f32_16x16x32_bf16 v[204:207], a[250:253], a[60:63], v[204:207]\n"
    "v_cvt_pk_bf16_f32 v196, v196, v197\n"
    "v_accvgpr_write_b32 a58, v196\n"
    "v_cvt_pk_bf16_f32 v197, v198, v199\n"
    "v_accvgpr_write_b32 a59, v197\n"
    "v_mfma_f32_16x16x32_bf16 v[0:3], a[64:67], a[0:3], 0\n"
    "v_cvt_pk_bf16_f32 v200, v200, v201\n"
    "v_cvt_pk_bf16_f32 v201, v202, v203\n"
    "v_mfma_f32_16x16x32_bf16 v[0:3], a[68:71], a[4:7], v[0:3]\n"
    "ds_read_b128 a[180:183], v234\n"
    "v_mfma_f32_16x16x32_bf16 v[0:3], a[72:75], a[8:11], v[0:3]\n"
    "v_accvgpr_write_b32 a60, v200\n"
    "v_mfma_f32_16x16x32_bf16 v[0:3], a[76:79], a[12:15], v[0:3]\n"
    "v_cvt_pk_bf16_f32 v204, v204, v205\n"
    "v_add_u32 v243, 1, v243\n"
    "v_mfma_f32_16x16x32_bf16 v[4:7], a[64:67], a[16:19], 0\n"
    "v_cvt_pk_bf16_f32 v205, v206, v207\n"
    "v_accvgpr_write_b32 a63, v205\n"
    "v_mfma_f32_16x16x32_bf16 v[4:7], a[68:71], a[20:23], v[4:7]\n"
    "s_waitcnt lgkmcnt(0)\n"
    "buffer_store_dwordx4 a[180:183], v235, s[48:51], s74 offen nt\n"
    "v_mfma_f32_16x16x32_bf16 v[4:7], a[72:75], a[24:27], v[4:7]\n"
    "v_max3_f32 v217, v0, v1, v2\n"
    "v_max_f32 v217, v217, v3\n"
    "v_mfma_f32_16x16x32_bf16 v[4:7], a[76:79], a[28:31], v[4:7]\n"
    "ds_bpermute_b32 v217, v236, v217\n"
    "v_mfma_f32_16x16x32_bf16 v[8:11], a[64:67], a[32:35], 0\n"
    "ds_read_b128 a[184:187], v234 offset:1024\n"
    "v_accvgpr_write_b32 a62, v204\n"
    "v_mfma_f32_16x16x32_bf16 v[8:11], a[68:71], a[36:39], v[8:11]\n"
    "v_accvgpr_write_b32 a61, v201\n"
    "v_add_u32 v234, 2048, v234\n"
    "v_mfma_f32_16x16x32_bf16 v[8:11], a[72:75], a[40:43], v[8:11]\n"
    "v_max3_f32 v219, v4, v5, v6\n"
    "v_max_f32 v219, v219, v7\n"
    "v_mfma_f32_16x16x32_bf16 v[8:11], a[76:79], a[44:47], v[8:11]\n"
    "s_waitcnt lgkmcnt(1)\n"
    "ds_bpermute_b32 v219, v236, v219\n"
    "v_sub_f32 v208, 0, v217\n"
    "v_sub_f32 v210, 0, v217\n"
    "v_sub_f32 v211, 0, v217\n"
    "v_mfma_f32_16x16x32_bf16 v[12:15], a[64:67], a[48:51], 0\n"
    "v_sub_f32 v209, 0, v217\n"
    "v_add_f32 v3, v208, v3\n"
    "v_mfma_f32_16x16x32_bf16 v[12:15], a[68:71], a[52:55], v[12:15]\n"
    "s_waitcnt lgkmcnt(1)\n"
    "buffer_store_dwordx4 a[184:187], v235, s[48:51], s74 offen offset:2048 nt\n"
    "v_exp_f32 v3, v3\n"
    "v_mfma_f32_16x16x32_bf16 v[12:15], a[72:75], a[56:59], v[12:15]\n"
    "v_max3_f32 v221, v8, v9, v10\n"
    "v_max_f32 v221, v221, v11\n"
    "v_mfma_f32_16x16x32_bf16 v[12:15], a[76:79], a[60:63], v[12:15]\n"
    "s_waitcnt lgkmcnt(0)\n"
    "ds_bpermute_b32 v221, v236, v221\n"
    "v_sub_f32 v214, 0, v219\n"
    "v_mfma_f32_16x16x32_bf16 v[192:195], a[80:83], a[0:3], v[208:211]\n"
    "v_add_f32 v1, v208, v1\n"
    "v_sub_f32 v212, 0, v219\n"
    "v_mfma_f32_16x16x32_bf16 v[192:195], a[84:87], a[4:7], v[192:195]\n"
    "v_add_f32 v0, v208, v0\n"
    "v_add_f32 v5, v212, v5\n"
    "v_mfma_f32_16x16x32_bf16 v[192:195], a[88:91], a[8:11], v[192:195]\n"
    "v_add_f32 v6, v212, v6\n"
    "v_max3_f32 v223, v12, v13, v14\n"
    "v_max_f32 v223, v223, v15\n"
    "v_mfma_f32_16x16x32_bf16 v[192:195], a[92:95], a[12:15], v[192:195]\n"
    "ds_bpermute_b32 v223, v236, v223\n"
    "v_sub_f32 v215, 0, v219\n"
    "v_mfma_f32_16x16x32_bf16 v[196:199], a[96:99], a[0:3], v[208:211]\n"
    "s_waitcnt vmcnt(6) lgkmcnt(1)\n"
    "ds_write_b32 v240, v243\n"
    "v_sub_f32 v248, s61, v221\n"
    "v_mfma_f32_16x16x32_bf16 v[196:199], a[100:103], a[4:7], v[196:199]\n"
    "s_mov_b32 m0, s73\n"
    "v_exp_f32 v1, v1\n"
    "v_add_f32 v8, v248, v8\n"
    "v_add_u32 v224, s60, v224\n"
    "v_mfma_f32_16x16x32_bf16 v[196:199], a[104:107], a[8:11], v[196:199]\n"
    "v_sub_f32 v213, 0, v219\n"
    "v_add_f32 v11, v248, v11\n"
    "v_exp_f32 v11, v11\n"
    "v_mfma_f32_16x16x32_bf16 v[196:199], a[108:111], a[12:15], v[196:199]\n"
    "s_waitcnt lgkmcnt(1)\n"
    "v_add_f32 v2, v208, v2\n"
    "v_add_f32 v7, v212, v7\n"
    "v_sub_f32 v250, s61, v221\n"
    "v_sub_f32 v252, s61, v223\n"
    "v_mfma_f32_16x16x32_bf16 v[200:203], a[112:115], a[0:3], v[208:211]\n"
    "buffer_load_dwordx4 v227, s[44:47], 0 offen nt lds\n"
    "v_exp_f32 v2, v2\n"
    "v_exp_f32 v192, v192\n"
    "v_exp_f32 v193, v193\n"
    "v_mfma_f32_16x16x32_bf16 v[200:203], a[116:119], a[4:7], v[200:203]\n"
    "v_add_f32 v4, v212, v4\n"
    "v_add_f32 v14, v252, v14\n"
    "v_mfma_f32_16x16x32_bf16 v[200:203], a[120:123], a[8:11], v[200:203]\n"
    "v_exp_f32 v0, v0\n"
    "v_exp_f32 v5, v5\n"
    "v_exp_f32 v6, v6\n"
    "v_add_f32 v9, v248, v9\n"
    "v_sub_f32 v255, s61, v223\n"
    "v_cvt_pk_bf16_f32 v130, v192, v193\n"
    "v_mfma_f32_16x16x32_bf16 v[200:203], a[124:127], a[12:15], v[200:203]\n"
    "v_add_f32 v10, v248, v10\n"
    "v_exp_f32 v199, v199\n"
    "v_mfma_f32_16x16x32_bf16 v[204:207], a[80:83], a[16:19], v[212:215]\n"
    "v_cvt_pk_bf16_f32 v129, v2, v3\n"
    "v_exp_f32 v7, v7\n"
    "v_exp_f32 v14, v14\n"
    "v_mfma_f32_16x16x32_bf16 v[204:207], a[84:87], a[20:23], v[204:207]\n"
    "v_exp_f32 v4, v4\n"
    "v_sub_f32 v251, s61, v221\n"
    "v_add_f32 v15, v252, v15\n"
    "v_mfma_f32_16x16x32_bf16 v[204:207], a[88:91], a[24:27], v[204:207]\n"
    "buffer_load_dwordx4 v227, s[44:47], 0 offen offset:1024 nt lds\n"
    "v_cvt_pk_bf16_f32 v128, v0, v1\n"
    "v_cvt_pk_bf16_f32 v136, v4, v5\n"
    "v_exp_f32 v15, v15\n"
    "v_mfma_f32_16x16x32_bf16 v[204:207], a[92:95], a[28:31], v[204:207]\n"
    "s_add_u32 s73, s73, 2048\n"
    "v_cvt_pk_bf16_f32 v137, v6, v7\n"
    "v_exp_f32 v8, v8\n"
    "v_exp_f32 v202, v202\n"
    "v_mfma_f32_16x16x32_bf16 v[0:3], a[96:99], a[16:19], v[212:215]\n"
    "s_add_u32 s44, s44, 2048\n"
    "v_exp_f32 v198, v198\n"
    "v_mfma_f32_16x16x32_bf16 v[0:3], a[100:103], a[20:23], v[0:3]\n"
    "s_addc_u32 s45, s45, 0\n"
    "v_add_f32 v12, v252, v12\n"
    "v_exp_f32 v194, v194\n"
    "v_exp_f32 v195, v195\n"
    "v_exp_f32 v203, v203\n"
    "v_mfma_f32_16x16x32_bf16 v[0:3], a[104:107], a[24:27], v[0:3]\n"
    "s_sub_i32 s46, s46, 2048\n"
    "v_sub_f32 v249, s61, v221\n"
    "v_add_f32 v13, v252, v13\n"
    "v_mfma_f32_16x16x32_bf16 v[0:3], a[108:111], a[28:31], v[0:3]\n"
    "s_max_i32 s46, s46, 0\n"
    "v_exp_f32 v10, v10\n"
    "v_exp_f32 v196, v196\n"
    "v_mfma_f32_16x16x32_bf16 v[4:7], a[112:115], a[16:19], v[212:215]\n"
    "s_add_u32 s74, s74, 4096\n"
    "v_exp_f32 v9, v9\n"
    "v_exp_f32 v197, v197\n"
    "v_mfma_f32_16x16x32_bf16 v[4:7], a[116:119], a[20:23], v[4:7]\n"
    "s_add_u32 s75, s75, 0x40000000\n"
    "v_exp_f32 v12, v12\n"
    "v_exp_f32 v13, v13\n"
    "v_exp_f32 v204, v204\n"
    "v_mfma_f32_16x16x32_bf16 v[4:7], a[120:123], a[24:27], v[4:7]\n"
    "s_cselect_b32 s71, 16256, 0\n"
    "v_cvt_pk_bf16_f32 v145, v10, v11\n"
    "v_exp_f32 v201, v201\n"
    "v_mfma_f32_16x16x32_bf16 v[4:7], a[124:127], a[28:31], v[4:7]\n"
    "s_sub_u32 s74, s74, s71\n"
    "ds_read_b32 v242, v241\n"
    "v_cvt_pk_bf16_f32 v144, v8, v9\n"
    "v_cvt_pk_bf16_f32 v152, v12, v13\n"
    "v_mfma_f32_16x16x32_bf16 v[8:11], a[80:83], a[32:35], v[248:251]\n"
    "s_mov_b32 s9, s46\n"
    "v_cvt_pk_bf16_f32 v131, v194, v195\n"
    "v_cvt_pk_bf16_f32 v135, v202, v203\n"
    "v_exp_f32 v206, v206\n"
    "v_mfma_f32_16x16x32_bf16 v[8:11], a[84:87], a[36:39], v[8:11]\n"
    "v_exp_f32 v200, v200\n"
    "v_mfma_f32_16x16x32_bf16 v[8:11], a[88:91], a[40:43], v[8:11]\n"
    "v_exp_f32 v1, v1\n"
    "v_mfma_f32_16x16x32_bf16 v[8:11], a[92:95], a[44:47], v[8:11]\n"
    "v_cvt_pk_bf16_f32 v153, v14, v15\n"
    "v_cvt_pk_bf16_f32 v133, v198, v199\n"
    "v_mfma_f32_16x16x32_bf16 v[12:15], a[96:99], a[32:35], v[248:251]\n"
    "s_add_u32 m0, s80, 16384\n"
    "v_sub_f32 v254, s61, v223\n"
    "v_cvt_pk_bf16_f32 v132, v196, v197\n"
    "v_mfma_f32_16x16x32_bf16 v[12:15], a[100:103], a[36:39], v[12:15]\n"
    "s_waitcnt lgkmcnt(0)\n"
    "v_exp_f32 v207, v207\n"
    "v_cmp_gt_u32 vcc, v243, v242\n"
    "v_mfma_f32_16x16x32_bf16 v[12:15], a[104:107], a[40:43], v[12:15]\n"
    "s_cbranch_vccnz LSPP2%=\n"
    "LSRP2%=:\n"
    "ds_read_b64_tr_b16 a[138:139], v225 offset:4352\n"
    "v_sub_f32 v253, s61, v223\n"
    "v_cvt_pk_bf16_f32 v139, v206, v207\n"
    "v_exp_f32 v8, v8\n"
    "v_mfma_f32_16x16x32_bf16 v[12:15], a[108:111], a[44:47], v[12:15]\n"
    "ds_read_b128 a[76:79], v224 offset:768\n"
    "v_exp_f32 v10, v10\n"
    "v_mfma_f32_16x16x32_bf16 v[192:195], a[112:115], a[32:35], v[248:251]\n"
    "buffer_load_dwordx4 v229, s[40:43], 0 offen nt lds\n"
    "v_cvt_pk_bf16_f32 v134, v200, v201\n"
    "v_mfma_f32_16x16x32_bf16 v[192:195], a[116:119], a[36:39], v[192:195]\n"
    "ds_read_b64_tr_b16 a[130:131], v225 offset:4096\n"
    "v_exp_f32 v6, v6\n"
    "v_mfma_f32_16x16x32_bf16 v[192:195], a[120:123], a[40:43], v[192:195]\n"
    "ds_read_b64_tr_b16 a[140:141], v225 offset:8448\n"
    "v_exp_f32 v4, v4\n"
    "v_exp_f32 v5, v5\n"
    "v_mfma_f32_16x16x32_bf16 v[192:195], a[124:127], a[44:47], v[192:195]\n"
    "buffer_load_dwordx4 v229, s[40:43], 0 offen offset:2048 nt lds\n"
    "v_exp_f32 v2, v2\n"
    "v_exp_f32 v3, v3\n"
    "v_exp_f32 v11, v11\n"
    "v_mfma_f32_16x16x32_bf16 v[196:199], a[80:83], a[48:51], v[252:255]\n"
    "s_add_u32 m0, s80, 17280\n"
    "ds_read_b64_tr_b16 a[128:129], v225 offset:0\n"
    "v_exp_f32 v205, v205\n"
    "v_exp_f32 v0, v0\n"
    "v_mfma_f32_16x16x32_bf16 v[196:199], a[84:87], a[52:55], v[196:199]\n"
    "v_exp_f32 v15, v15\n"
    "v_mfma_f32_16x16x32_bf16 v[196:199], a[88:91], a[56:59], v[196:199]\n"
    "buffer_load_dwordx4 v229, s[40:43], 0 offen offset:128 nt lds\n"
    "ds_read_b64_tr_b16 a[136:137], v225 offset:256\n"
    "v_exp_f32 v9, v9\n"
    "v_exp_f32 v14, v14\n"
    "v_mfma_f32_16x16x32_bf16 v[196:199], a[92:95], a[60:63], v[196:199]\n"
    "ds_read_b64_tr_b16 a[142:143], v225 offset:12544\n"
    "v_exp_f32 v13, v13\n"
    "v_mfma_f32_16x16x32_bf16 v[200:203], a[96:99], a[48:51], v[252:255]\n"
    "ds_read_b128 a[64:67], v224 offset:0\n"
    "ds_read_b64_tr_b16 a[132:133], v225 offset:8192\n"
    "v_exp_f32 v195, v195\n"
    "v_mfma_f32_16x16x32_bf16 v[200:203], a[100:103], a[52:55], v[200:203]\n"
    "buffer_load_dwordx4 v229, s[40:43], 0 offen offset:2176 nt lds\n"
    "ds_read_b64_tr_b16 a[134:135], v225 offset:12288\n"
    "v_cvt_pk_bf16_f32 v138, v204, v205\n"
    "v_exp_f32 v7, v7\n"
    "v_exp_f32 v12, v12\n"
    "v_mfma_f32_16x16x32_bf16 v[200:203], a[104:107], a[56:59], v[200:203]\n"
    "v_cvt_pk_bf16_f32 v140, v0, v1\n"
    "v_cvt_pk_bf16_f32 v146, v8, v9\n"
    "v_exp_f32 v192, v192\n"
    "v_exp_f32 v193, v193\n"
    "v_exp_f32 v199, v199\n"
    "v_mfma_f32_16x16x32_bf16 v[200:203], a[108:111], a[60:63], v[200:203]\n"
    "v_cvt_pk_bf16_f32 v142, v4, v5\n"
    "v_exp_f32 v197, v197\n"
    "v_exp_f32 v198, v198\n"
    "v_mfma_f32_16x16x32_bf16 v[204:207], a[112:115], a[48:51], v[252:255]\n"
    "ds_read_b128 a[68:71], v224 offset:256\n"
    "ds_read_b128 a[72:75], v224 offset:512\n"
    "v_exp_f32 v194, v194\n"
    "v_cvt_pk_bf16_f32 v150, v192, v193\n"
    "v_mfma_f32_16x16x32_bf16 v[204:207], a[116:119], a[52:55], v[204:207]\n"
    "v_cvt_pk_bf16_f32 v141, v2, v3\n"
    "v_cvt_pk_bf16_f32 v143, v6, v7\n"
    "v_cvt_pk_bf16_f32 v149, v14, v15\n"
    "v_cvt_pk_bf16_f32 v155, v198, v199\n"
    "v_exp_f32 v200, v200\n"
    "v_exp_f32 v201, v201\n"
    "v_exp_f32 v203, v203\n"
    "v_mfma_f32_16x16x32_bf16 v[204:207], a[120:123], a[56:59], v[204:207]\n"
    "v_cvt_pk_bf16_f32 v148, v12, v13\n"
    "v_exp_f32 v196, v196\n"
    "v_exp_f32 v202, v202\n"
    "v_cvt_pk_bf16_f32 v156, v200, v201\n"
    "v_mfma_f32_16x16x32_bf16 v[204:207], a[124:127], a[60:63], v[204:207]\n"
    "s_waitcnt lgkmcnt(4)\n"
    "v_cvt_pk_bf16_f32 v147, v10, v11\n"
    "v_cvt_pk_bf16_f32 v151, v194, v195\n"
    "v_cvt_pk_bf16_f32 v154, v196, v197\n"
    "v_cvt_pk_bf16_f32 v157, v202, v203\n"
    "s_branch LT0%=\n"
    ".p2align 6\n"
    "LTO2%=:\n"
    "v_mfma_f32_16x16x32_bf16 v[192:195], a[64:67], a[0:3], v[208:211]\n"
    "v_exp_f32 v204, v204\n"
    "v_mfma_f32_16x16x32_bf16 v[192:195], a[68:71], a[4:7], v[192:195]\n"
    "s_add_u32 s58, s58, 16384\n"
    "v_exp_f32 v206, v206\n"
    "v_mfma_f32_16x16x32_bf16 v[192:195], a[72:75], a[8:11], v[192:195]\n"
    "s_cmp_eq_u32 s58, 49152\n"
    "v_exp_f32 v207, v207\n"
    "v_mfma_f32_16x16x32_bf16 v[192:195], a[76:79], a[12:15], v[192:195]\n"
    "s_waitcnt lgkmcnt(2)\n"
    "v_exp_f32 v205, v205\n"
    "v_mfma_f32_16x16x32_bf16 v[0:3], a[128:131], v[160:163], v[0:3]\n"
    "s_waitcnt lgkmcnt(0)\n"
    "ds_read_b128 a[80:83], v224 offset:4096\n"
    "ds_read_b64_tr_b16 a[158:159], v225 offset:29440\n"
    "v_mfma_f32_16x16x32_bf16 v[0:3], a[132:135], v[164:167], v[0:3]\n"
    "s_cselect_b32 s60, s59, s72\n"
    "v_mfma_f32_16x16x32_bf16 v[4:7], a[128:131], v[168:171], v[4:7]\n"
    "s_cselect_b32 s58, 0, s58\n"
    "ds_read_b64_tr_b16 a[232:233], v238 offset:37120\n"
    "v_mfma_f32_16x16x32_bf16 v[4:7], a[132:135], v[172:175], v[4:7]\n"
    "v_exp_f32 v192, v192\n"
    "v_mfma_f32_16x16x32_bf16 v[196:199], a[64:67], a[16:19], v[212:215]\n"
    "ds_read_b128 a[84:87], v224 offset:4352\n"
    "v_mfma_f32_16x16x32_bf16 v[196:199], a[68:71], a[20:23], v[196:199]\n"
    "v_exp_f32 v193, v193\n"
    "v_mfma_f32_16x16x32_bf16 v[196:199], a[72:75], a[24:27], v[196:199]\n"
    "ds_read_b64_tr_b16 a[146:147], v225 offset:20992\n"
    "v_mfma_f32_16x16x32_bf16 v[196:199], a[76:79], a[28:31], v[196:199]\n"
    "v_mfma_f32_16x16x32_bf16 v[8:11], a[128:131], v[176:179], v[8:11]\n"
    "v_cvt_pk_bf16_f32 v190, v204, v205\n"
    "v_cvt_pk_bf16_f32 v128, v192, v193\n"
    "v_mfma_f32_16x16x32_bf16 v[8:11], a[132:135], v[180:183], v[8:11]\n"
    "v_cvt_pk_bf16_f32 v191, v206, v207\n"
    "v_mfma_f32_16x16x32_bf16 v[12:15], a[128:131], v[184:187], v[12:15]\n"
    "ds_read_b128 a[180:183], v234\n"
    "v_mfma_f32_16x16x32_bf16 v[12:15], a[132:135], v[188:191], v[12:15]\n"
    "ds_read_b128 a[92:95], v224 offset:4864\n"
    "ds_read_b64_tr_b16 a[154:155], v225 offset:21248\n"
    "v_mfma_f32_16x16x32_bf16 v[200:203], a[64:67], a[32:35], v[248:251]\n"
    "v_exp_f32 v194, v194\n"
    "v_mfma_f32_16x16x32_bf16 v[200:203], a[68:71], a[36:39], v[200:203]\n"
    "ds_read_b128 a[88:91], v224 offset:4608\n"
    "v_mfma_f32_16x16x32_bf16 v[200:203], a[72:75], a[40:43], v[200:203]\n"
    "v_exp_f32 v195, v195\n"
    "v_mfma_f32_16x16x32_bf16 v[200:203], a[76:79], a[44:47], v[200:203]\n"
    "v_mfma_f32_16x16x32_bf16 v[16:19], a[136:139], v[160:163], v[16:19]\n"
    "s_waitcnt lgkmcnt(3)\n"
    "buffer_store_dwordx4 a[180:183], v235, s[48:51], s74 offen nt\n"
    "v_mfma_f32_16x16x32_bf16 v[16:19], a[140:143], v[164:167], v[16:19]\n"
    "ds_read_b64_tr_b16 a[216:217], v238 offset:36864\n"
    "v_mfma_f32_16x16x32_bf16 v[20:23], a[136:139], v[168:171], v[20:23]\n"
    "v_cvt_pk_bf16_f32 v129, v194, v195\n"
    "v_mfma_f32_16x16x32_bf16 v[20:23], a[140:143], v[172:175], v[20:23]\n"
    "v_exp_f32 v196, v196\n"
    "v_smfmac_f32_16x16x64_bf16 a[160:163], a[176:179], v[160:167], v237\n"
    "ds_read_b64_tr_b16 a[148:149], v225 offset:25088\n"
    "v_mfma_f32_16x16x32_bf16 v[204:207], a[64:67], a[48:51], v[252:255]\n"
    "v_exp_f32 v198, v198\n"
    "v_mfma_f32_16x16x32_bf16 v[204:207], a[68:71], a[52:55], v[204:207]\n"
    "ds_read_b64_tr_b16 a[144:145], v225 offset:16896\n"
    "ds_read_b64_tr_b16 a[150:151], v225 offset:29184\n"
    "v_mfma_f32_16x16x32_bf16 v[204:207], a[72:75], a[56:59], v[204:207]\n"
    "v_exp_f32 v201, v201\n"
    "v_mfma_f32_16x16x32_bf16 v[204:207], a[76:79], a[60:63], v[204:207]\n"
    "v_exp_f32 v200, v200\n"
    "v_mfma_f32_16x16x32_bf16 v[24:27], a[136:139], v[176:179], v[24:27]\n"
    "v_exp_f32 v203, v203\n"
    "v_mfma_f32_16x16x32_bf16 v[24:27], a[140:143], v[180:183], v[24:27]\n"
    "v_exp_f32 v199, v199\n"
    "v_mfma_f32_16x16x32_bf16 v[28:31], a[136:139], v[184:187], v[28:31]\n"
    "ds_read_b64_tr_b16 a[134:135], v225 offset:29696\n"
    "v_cvt_pk_bf16_f32 v144, v200, v201\n"
    "v_mfma_f32_16x16x32_bf16 v[28:31], a[140:143], v[188:191], v[28:31]\n"
    "ds_read_b128 a[64:67], v224 offset:8192\n"
    "ds_read_b64_tr_b16 a[132:133], v225 offset:25600\n"
    "v_mfma_f32_16x16x32_bf16 v[192:195], a[80:83], a[0:3], v[208:211]\n"
    "v_exp_f32 v205, v205\n"
    "v_mfma_f32_16x16x32_bf16 v[192:195], a[84:87], a[4:7], v[192:195]\n"
    "s_waitcnt lgkmcnt(7)\n"
    "ds_read_b128 a[76:79], v224 offset:8960\n"
    "ds_read_b64_tr_b16 a[152:153], v225 offset:17152\n"
    "v_mfma_f32_16x16x32_bf16 v[192:195], a[88:91], a[8:11], v[192:195]\n"
    "v_exp_f32 v197, v197\n"
    "v_mfma_f32_16x16x32_bf16 v[192:195], a[92:95], a[12:15], v[192:195]\n"
    "s_waitcnt lgkmcnt(6)\n"
    "v_exp_f32 v207, v207\n"
    "v_mfma_f32_16x16x32_bf16 v[32:35], a[144:147], v[160:163], v[32:35]\n"
    "s_waitcnt lgkmcnt(5)\n"
    "v_cvt_pk_bf16_f32 v137, v198, v199\n"
    "v_mfma_f32_16x16x32_bf16 v[32:35], a[148:151], v[164:167], v[32:35]\n"
    "v_exp_f32 v202, v202\n"
    "v_mfma_f32_16x16x32_bf16 v[36:39], a[144:147], v[168:171], v[36:39]\n"
    "ds_read_b64_tr_b16 a[128:129], v225 offset:17408\n"
    "v_cvt_pk_bf16_f32 v136, v196, v197\n"
    "v_mfma_f32_16x16x32_bf16 v[36:39], a[148:151], v[172:175], v[36:39]\n"
    "ds_read_b128 a[184:187], v234 offset:1024\n"
    "v_cvt_pk_bf16_f32 v145, v202, v203\n"
    "v_mfma_f32_16x16x32_bf16 v[196:199], a[80:83], a[16:19], v[212:215]\n"
    "v_exp_f32 v192, v192\n"
    "v_mfma_f32_16x16x32_bf16 v[196:199], a[84:87], a[20:23], v[196:199]\n"
    "ds_read_b64_tr_b16 a[230:231], v238 offset:20736\n"
    "v_mfma_f32_16x16x32_bf16 v[196:199], a[88:91], a[24:27], v[196:199]\n"
    "v_exp_f32 v204, v204\n"
    "v_mfma_f32_16x16x32_bf16 v[196:199], a[92:95], a[28:31], v[196:199]\n"
    "v_exp_f32 v206, v206\n"
    "v_mfma_f32_16x16x32_bf16 v[40:43], a[144:147], v[176:179], v[40:43]\n"
    "ds_read_b64_tr_b16 a[130:131], v225 offset:21504\n"
    "v_add_u32 v234, 2048, v234\n"
    "v_mfma_f32_16x16x32_bf16 v[40:43], a[148:151], v[180:183], v[40:43]\n"
    "s_waitcnt lgkmcnt(2)\n"
    "buffer_store_dwordx4 a[184:187], v235, s[48:51], s74 offen offset:2048 nt\n"
    "v_mfma_f32_16x16x32_bf16 v[44:47], a[144:147], v[184:187], v[44:47]\n"
    "s_add_u32 s74, s74, 4096\n"
    "ds_read_b128 a[72:75], v224 offset:8704\n"
    "ds_read_b64_tr_b16 a[156:157], v225 offset:25344\n"
    "v_mfma_f32_16x16x32_bf16 v[44:47], a[148:151], v[188:191], v[44:47]\n"
    "v_exp_f32 v193, v193\n"
    "v_mfma_f32_16x16x32_bf16 v[200:203], a[80:83], a[32:35], v[248:251]\n"
    "ds_read_b64_tr_b16 a[214:215], v238 offset:20480\n"
    "v_cvt_pk_bf16_f32 v152, v204, v205\n"
    "v_mfma_f32_16x16x32_bf16 v[200:203], a[84:87], a[36:39], v[200:203]\n"
    "ds_read_b128 a[68:71], v224 offset:8448\n"
    "v_cvt_pk_bf16_f32 v153, v206, v207\n"
    "v_mfma_f32_16x16x32_bf16 v[200:203], a[88:91], a[40:43], v[200:203]\n"
    "v_exp_f32 v197, v197\n"
    "v_mfma_f32_16x16x32_bf16 v[200:203], a[92:95], a[44:47], v[200:203]\n"
    "ds_read_b64_tr_b16 a[144:145], v225 offset:17920\n"
    "ds_read_b64_tr_b16 a[238:239], v239 offset:20736\n"
    "v_mfma_f32_16x16x32_bf16 v[48:51], a[152:155], v[160:163], v[48:51]\n"
    "s_waitcnt lgkmcnt(4)\n"
    "v_exp_f32 v198, v198\n"
    "v_mfma_f32_16x16x32_bf16 v[48:51], a[156:159], v[164:167], v[48:51]\n"
    "ds_read_b64_tr_b16 a[146:147], v225 offset:22016\n"
    "ds_read_b64_tr_b16 a[150:151], v225 offset:30208\n"
    "v_mfma_f32_16x16x32_bf16 v[52:55], a[152:155], v[168:171], v[52:55]\n"
    "v_exp_f32 v194, v194\n"
    "v_mfma_f32_16x16x32_bf16 v[52:55], a[156:159], v[172:175], v[52:55]\n"
    "v_exp_f32 v199, v199\n"
    "v_smfmac_f32_16x16x64_bf16 a[164:167], a[176:179], v[168:175], v237\n"
    "ds_read_b64_tr_b16 a[218:219], v238 offset:53248\n"
    "v_mfma_f32_16x16x32_bf16 v[204:207], a[80:83], a[48:51], v[252:255]\n"
    "ds_read_b64_tr_b16 a[140:141], v225 offset:25856\n"
    "v_cvt_pk_bf16_f32 v130, v192, v193\n"
    "v_mfma_f32_16x16x32_bf16 v[204:207], a[84:87], a[52:55], v[204:207]\n"
    "v_exp_f32 v195, v195\n"
    "v_mfma_f32_16x16x32_bf16 v[204:207], a[88:91], a[56:59], v[204:207]\n"
    "v_exp_f32 v203, v203\n"
    "v_mfma_f32_16x16x32_bf16 v[204:207], a[92:95], a[60:63], v[204:207]\n"
    "ds_read_b64_tr_b16 a[148:149], v225 offset:26112\n"
    "v_cvt_pk_bf16_f32 v131, v194, v195\n"
    "v_mfma_f32_16x16x32_bf16 v[56:59], a[152:155], v[176:179], v[56:59]\n"
    "ds_read_b128 a[88:91], v224 offset:12800\n"
    "v_cvt_pk_bf16_f32 v139, v198, v199\n"
    "v_mfma_f32_16x16x32_bf16 v[56:59], a[156:159], v[180:183], v[56:59]\n"
    "ds_read_b64_tr_b16 a[136:137], v225 offset:17664\n"
    "ds_read_b64_tr_b16 a[138:139], v225 offset:21760\n"
    "v_mfma_f32_16x16x32_bf16 v[60:63], a[152:155], v[184:187], v[60:63]\n"
    "v_exp_f32 v196, v196\n"
    "v_mfma_f32_16x16x32_bf16 v[60:63], a[156:159], v[188:191], v[60:63]\n"
    "ds_read_b64_tr_b16 a[152:153], v225 offset:18176\n"
    "ds_read_b64_tr_b16 a[156:157], v225 offset:26368\n"
    "v_mfma_f32_16x16x32_bf16 v[192:195], a[64:67], a[0:3], v[208:211]\n"
    "s_waitcnt lgkmcnt(12)\n"
    "ds_read_b128 a[92:95], v224 offset:13056\n"
    "v_mfma_f32_16x16x32_bf16 v[192:195], a[68:71], a[4:7], v[192:195]\n"
    "v_exp_f32 v201, v201\n"
    "v_mfma_f32_16x16x32_bf16 v[192:195], a[72:75], a[8:11], v[192:195]\n"
    "v_exp_f32 v206, v206\n"
    "v_mfma_f32_16x16x32_bf16 v[192:195], a[76:79], a[12:15], v[192:195]\n"
    "ds_read_b128 a[80:83], v224 offset:12288\n"
    "ds_read_b128 a[84:87], v224 offset:12544\n"
    "v_mfma_f32_16x16x32_bf16 v[64:67], a[128:131], v[160:163], v[64:67]\n"
    "ds_read_b64_tr_b16 a[154:155], v225 offset:22272\n"
    "ds_read_b64_tr_b16 a[158:159], v225 offset:30464\n"
    "v_mfma_f32_16x16x32_bf16 v[64:67], a[132:135], v[164:167], v[64:67]\n"
    "v_exp_f32 v204, v204\n"
    "v_mfma_f32_16x16x32_bf16 v[68:71], a[128:131], v[168:171], v[68:71]\n"
    "ds_read_b64_tr_b16 a[142:143], v225 offset:29952\n"
    "v_add_u32 v243, 1, v243\n"
    "v_mfma_f32_16x16x32_bf16 v[68:71], a[132:135], v[172:175], v[68:71]\n"
    "s_waitcnt vmcnt(2)\n"
    "ds_write_b32 v240, v243\n"
    "v_cvt_pk_bf16_f32 v138, v196, v197\n"
    "v_mfma_f32_16x16x32_bf16 v[196:199], a[64:67], a[16:19], v[212:215]\n"
    "v_exp_f32 v200, v200\n"
    "v_mfma_f32_16x16x32_bf16 v[196:199], a[68:71], a[20:23], v[196:199]\n"
    "v_exp_f32 v202, v202\n"
    "v_mfma_f32_16x16x32_bf16 v[196:199], a[72:75], a[24:27], v[196:199]\n"
    "v_exp_f32 v193, v193\n"
    "v_mfma_f32_16x16x32_bf16 v[196:199], a[76:79], a[28:31], v[196:199]\n"
    "v_exp_f32 v192, v192\n"
    "v_mfma_f32_16x16x32_bf16 v[72:75], a[128:131], v[176:179], v[72:75]\n"
    "v_cvt_pk_bf16_f32 v146, v200, v201\n"
    "v_cvt_pk_bf16_f32 v147, v202, v203\n"
    "v_mfma_f32_16x16x32_bf16 v[72:75], a[132:135], v[180:183], v[72:75]\n"
    "v_exp_f32 v207, v207\n"
    "v_mfma_f32_16x16x32_bf16 v[76:79], a[128:131], v[184:187], v[76:79]\n"
    "ds_read_b64_tr_b16 a[228:229], v238 offset:4352\n"
    "v_cvt_pk_bf16_f32 v132, v192, v193\n"
    "v_mfma_f32_16x16x32_bf16 v[76:79], a[132:135], v[188:191], v[76:79]\n"
    "v_exp_f32 v205, v205\n"
    "v_mfma_f32_16x16x32_bf16 v[200:203], a[64:67], a[32:35], v[248:251]\n"
    "v_exp_f32 v199, v199\n"
    "v_mfma_f32_16x16x32_bf16 v[200:203], a[68:71], a[36:39], v[200:203]\n"
    "v_exp_f32 v194, v194\n"
    "v_mfma_f32_16x16x32_bf16 v[200:203], a[72:75], a[40:43], v[200:203]\n"
    "ds_read_b64_tr_b16 a[220:221], v239 offset:4096\n"
    "v_add_u32 v224, s60, v224\n"
    "v_mfma_f32_16x16x32_bf16 v[200:203], a[76:79], a[44:47], v[200:203]\n"
    "s_waitcnt lgkmcnt(11)\n"
    "v_exp_f32 v198, v198\n"
    "v_mfma_f32_16x16x32_bf16 v[80:83], a[136:139], v[160:163], v[80:83]\n"
    "s_waitcnt lgkmcnt(3)\n"
    "ds_read_b64_tr_b16 a[242:243], v239 offset:53504\n"
    "v_cvt_pk_bf16_f32 v154, v204, v205\n"
    "v_mfma_f32_16x16x32_bf16 v[80:83], a[140:143], v[164:167], v[80:83]\n"
    "ds_read_b64_tr_b16 a[224:225], v239 offset:36864\n"
    "ds_read_b64_tr_b16 a[240:241], v239 offset:37120\n"
    "v_mfma_f32_16x16x32_bf16 v[84:87], a[136:139], v[168:171], v[84:87]\n"
    "ds_read_b64_tr_b16 a[234:235], v238 offset:53504\n"
    "v_cvt_pk_bf16_f32 v155, v206, v207\n"
    "v_mfma_f32_16x16x32_bf16 v[84:87], a[140:143], v[172:175], v[84:87]\n"
    "v_exp_f32 v203, v203\n"
    "v_smfmac_f32_16x16x64_bf16 a[168:171], a[176:179], v[176:183], v237\n"
    "ds_read_b64_tr_b16 a[236:237], v239 offset:4352\n"
    "v_mfma_f32_16x16x32_bf16 v[204:207], a[64:67], a[48:51], v[252:255]\n"
    "v_exp_f32 v197, v197\n"
    "v_mfma_f32_16x16x32_bf16 v[204:207], a[68:71], a[52:55], v[204:207]\n"
    "ds_read_b32 v242, v241\n"
    "v_cvt_pk_bf16_f32 v141, v198, v199\n"
    "v_mfma_f32_16x16x32_bf16 v[204:207], a[72:75], a[56:59], v[204:207]\n"
    "v_exp_f32 v201, v201\n"
    "v_mfma_f32_16x16x32_bf16 v[204:207], a[76:79], a[60:63], v[204:207]\n"
    "v_exp_f32 v202, v202\n"
    "v_mfma_f32_16x16x32_bf16 v[88:91], a[136:139], v[176:179], v[88:91]\n"
    "v_exp_f32 v196, v196\n"
    "v_mfma_f32_16x16x32_bf16 v[88:91], a[140:143], v[180:183], v[88:91]\n"
    "v_exp_f32 v195, v195\n"
    "v_mfma_f32_16x16x32_bf16 v[92:95], a[136:139], v[184:187], v[92:95]\n"
    "ds_read_b64_tr_b16 a[212:213], v238 offset:4096\n"
    "v_cvt_pk_bf16_f32 v133, v194, v195\n"
    "v_mfma_f32_16x16x32_bf16 v[92:95], a[140:143], v[188:191], v[92:95]\n"
    "s_waitcnt lgkmcnt(1)\n"
    "ds_read_b64_tr_b16 a[226:227], v239 offset:53248\n"
    "v_cmp_gt_u32 vcc, v243, v242\n"
    "v_mfma_f32_16x16x32_bf16 v[192:195], a[80:83], a[0:3], v[208:211]\n"
    "s_cbranch_vccnz LSPTo2%=\n"
    "LSRTo2%=:\n"
    "ds_read_b128 a[108:111], v224 offset:4864\n"
    "ds_read_b128 a[120:123], v224 offset:8704\n"
    "v_mfma_f32_16x16x32_bf16 v[192:195], a[84:87], a[4:7], v[192:195]\n"
    "v_exp_f32 v204, v204\n"
    "v_mfma_f32_16x16x32_bf16 v[192:195], a[88:91], a[8:11], v[192:195]\n"
    "v_exp_f32 v205, v205\n"
    "v_mfma_f32_16x16x32_bf16 v[192:195], a[92:95], a[12:15], v[192:195]\n"
    "v_exp_f32 v207, v207\n"
    "v_mfma_f32_16x16x32_bf16 v[96:99], a[144:147], v[160:163], v[96:99]\n"
    "v_exp_f32 v206, v206\n"
    "v_mfma_f32_16x16x32_bf16 v[96:99], a[148:151], v[164:167], v[96:99]\n"
    "ds_read_b64_tr_b16 a[130:131], v238 offset:16384\n"
    "v_cvt_pk_bf16_f32 v140, v196, v197\n"
    "v_mfma_f32_16x16x32_bf16 v[100:103], a[144:147], v[168:171], v[100:103]\n"
    "v_exp_f32 v200, v200\n"
    "v_mfma_f32_16x16x32_bf16 v[100:103], a[148:151], v[172:175], v[100:103]\n"
    "ds_read_b64_tr_b16 a[128:129], v238 offset:0\n"
    "ds_read_b128 a[112:115], v224 offset:8192\n"
    "v_mfma_f32_16x16x32_bf16 v[196:199], a[80:83], a[16:19], v[212:215]\n"
    "v_exp_f32 v193, v193\n"
    "v_mfma_f32_16x16x32_bf16 v[196:199], a[84:87], a[20:23], v[196:199]\n"
    "ds_read_b128 a[100:103], v224 offset:4352\n"
    "v_cvt_pk_bf16_f32 v157, v206, v207\n"
    "v_mfma_f32_16x16x32_bf16 v[196:199], a[88:91], a[24:27], v[196:199]\n"
    "ds_read_b64_tr_b16 a[134:135], v238 offset:49152\n"
    "v_cvt_pk_bf16_f32 v156, v204, v205\n"
    "v_mfma_f32_16x16x32_bf16 v[196:199], a[92:95], a[28:31], v[196:199]\n"
    "v_exp_f32 v192, v192\n"
    "v_mfma_f32_16x16x32_bf16 v[104:107], a[144:147], v[176:179], v[104:107]\n"
    "ds_read_b128 a[68:71], v224 offset:256\n"
    "ds_read_b128 a[124:127], v224 offset:8960\n"
    "v_mfma_f32_16x16x32_bf16 v[104:107], a[148:151], v[180:183], v[104:107]\n"
    "ds_read_b128 a[76:79], v224 offset:768\n"
    "v_cvt_pk_bf16_f32 v148, v200, v201\n"
    "v_mfma_f32_16x16x32_bf16 v[108:111], a[144:147], v[184:187], v[108:111]\n"
    "ds_read_b128 a[72:75], v224 offset:512\n"
    "v_cvt_pk_bf16_f32 v149, v202, v203\n"
    "v_mfma_f32_16x16x32_bf16 v[108:111], a[148:151], v[188:191], v[108:111]\n"
    "v_exp_f32 v199, v199\n"
    "v_mfma_f32_16x16x32_bf16 v[200:203], a[80:83], a[32:35], v[248:251]\n"
    "v_exp_f32 v196, v196\n"
    "v_mfma_f32_16x16x32_bf16 v[200:203], a[84:87], a[36:39], v[200:203]\n"
    "v_exp_f32 v195, v195\n"
    "v_mfma_f32_16x16x32_bf16 v[200:203], a[88:91], a[40:43], v[200:203]\n"
    "v_exp_f32 v194, v194\n"
    "v_mfma_f32_16x16x32_bf16 v[200:203], a[92:95], a[44:47], v[200:203]\n"
    "ds_read_b64_tr_b16 a[142:143], v239 offset:49152\n"
    "ds_read_b64_tr_b16 a[222:223], v239 offset:20480\n"
    "v_mfma_f32_16x16x32_bf16 v[112:115], a[152:155], v[160:163], v[112:115]\n"
    "ds_read_b64_tr_b16 a[132:133], v238 offset:32768\n"
    "v_cvt_pk_bf16_f32 v134, v192, v193\n"
    "v_mfma_f32_16x16x32_bf16 v[112:115], a[156:159], v[164:167], v[112:115]\n"
    "v_exp_f32 v197, v197\n"
    "v_mfma_f32_16x16x32_bf16 v[116:119], a[152:155], v[168:171], v[116:119]\n"
    "ds_read_b64_tr_b16 a[136:137], v239 offset:0\n"
    "v_cvt_pk_bf16_f32 v142, v196, v197\n"
    "v_mfma_f32_16x16x32_bf16 v[116:119], a[156:159], v[172:175], v[116:119]\n"
    "ds_read_b128 a[64:67], v224 offset:0\n"
    "ds_read_b128 a[116:119], v224 offset:8448\n"
    "v_smfmac_f32_16x16x64_bf16 a[172:175], a[176:179], v[184:191], v237\n"
    "ds_read_b64_tr_b16 a[140:141], v239 offset:32768\n"
    "v_mfma_f32_16x16x32_bf16 v[204:207], a[80:83], a[48:51], v[252:255]\n"
    "ds_read_b128 a[104:107], v224 offset:4608\n"
    "v_cvt_pk_bf16_f32 v135, v194, v195\n"
    "v_mfma_f32_16x16x32_bf16 v[204:207], a[84:87], a[52:55], v[204:207]\n"
    "v_exp_f32 v200, v200\n"
    "v_mfma_f32_16x16x32_bf16 v[204:207], a[88:91], a[56:59], v[204:207]\n"
    "v_exp_f32 v202, v202\n"
    "v_mfma_f32_16x16x32_bf16 v[204:207], a[92:95], a[60:63], v[204:207]\n"
    "v_exp_f32 v198, v198\n"
    "v_mfma_f32_16x16x32_bf16 v[120:123], a[152:155], v[176:179], v[120:123]\n"
    "v_exp_f32 v201, v201\n"
    "v_mfma_f32_16x16x32_bf16 v[120:123], a[156:159], v[180:183], v[120:123]\n"
    "ds_read_b128 a[96:99], v224 offset:4096\n"
    "v_cvt_pk_bf16_f32 v143, v198, v199\n"
    "v_cvt_pk_bf16_f32 v150, v200, v201\n"
    "v_mfma_f32_16x16x32_bf16 v[124:127], a[152:155], v[184:187], v[124:127]\n"
    "v_exp_f32 v203, v203\n"
    "v_mfma_f32_16x16x32_bf16 v[124:127], a[156:159], v[188:191], v[124:127]\n"
    "s_waitcnt lgkmcnt(4)\n"
    "ds_read_b64_tr_b16 a[138:139], v239 offset:16384\n"
    "v_cvt_pk_bf16_f32 v151, v202, v203\n"
    ".p2align 6\n"
    "LP1L%=:\n"
    "v_mfma_f32_16x16x32_bf16 v[192:195], a[64:67], a[0:3], v[208:211]\n"
    "s_lshl_b32 s71, s81, 14\n"
    "ds_read_b128 a[80:83], v224 offset:12288\n"
    "ds_read_b128 a[84:87], v224 offset:12544\n"
    "v_mfma_f32_16x16x32_bf16 v[192:195], a[68:71], a[4:7], v[192:195]\n"
    "s_cmp_eq_u32 s81, 1\n"
    "ds_read_b128 a[92:95], v224 offset:13056\n"
    "v_exp_f32 v206, v206\n"
    "v_mfma_f32_16x16x32_bf16 v[192:195], a[72:75], a[8:11], v[192:195]\n"
    "s_cselect_b32 s71, 65536, s71\n"
    "ds_read_b64_tr_b16 a[150:151], v238 offset:49408\n"
    "ds_read_b64_tr_b16 a[200:201], v238 offset:33536\n"
    "v_mfma_f32_16x16x32_bf16 v[192:195], a[76:79], a[12:15], v[192:195]\n"
    "s_waitcnt lgkmcnt(6)\n"
    "v_exp_f32 v204, v204\n"
    "v_mfma_f32_16x16x32_bf16 v[196:199], a[96:99], a[0:3], v[208:211]\n"
    "s_sub_u32 s60, s71, s62\n"
    "ds_read_b128 a[88:91], v224 offset:12800\n"
    "v_exp_f32 v207, v207\n"
    "v_mfma_f32_16x16x32_bf16 v[196:199], a[100:103], a[4:7], v[196:199]\n"
    "s_sub_u32 s58, s60, 16384\n"
    "ds_read_b64_tr_b16 a[180:181], v238 offset:512\n"
    "v_exp_f32 v205, v205\n"
    "v_mfma_f32_16x16x32_bf16 v[196:199], a[104:107], a[8:11], v[196:199]\n"
    "v_exp_f32 v192, v192\n"
    "v_mfma_f32_16x16x32_bf16 v[196:199], a[108:111], a[12:15], v[196:199]\n"
    "v_exp_f32 v193, v193\n"
    "v_mfma_f32_16x16x32_bf16 v[200:203], a[112:115], a[0:3], v[208:211]\n"
    "ds_read_b128 a[244:247], v234\n"
    "v_cvt_pk_bf16_f32 v159, v206, v207\n"
    "v_mfma_f32_16x16x32_bf16 v[200:203], a[116:119], a[4:7], v[200:203]\n"
    "ds_read_b64_tr_b16 a[206:207], v239 offset:17152\n"
    "v_exp_f32 v195, v195\n"
    "v_mfma_f32_16x16x32_bf16 v[200:203], a[120:123], a[8:11], v[200:203]\n"
    "v_cvt_pk_bf16_f32 v158, v204, v205\n"
    "v_exp_f32 v194, v194\n"
    "v_mfma_f32_16x16x32_bf16 v[200:203], a[124:127], a[12:15], v[200:203]\n"
    "s_waitcnt lgkmcnt(8)\n"
    "v_cvt_pk_bf16_f32 v160, v192, v193\n"
    "v_exp_f32 v199, v199\n"
    "v_mfma_f32_16x16x32_bf16 v[204:207], a[80:83], a[0:3], v[208:211]\n"
    "s_waitcnt lgkmcnt(7)\n"
    "ds_read_b64_tr_b16 a[148:149], v238 offset:33024\n"
    "v_exp_f32 v196, v196\n"
    "v_exp_f32 v197, v197\n"
    "v_mfma_f32_16x16x32_bf16 v[204:207], a[84:87], a[4:7], v[204:207]\n"
    "s_waitcnt lgkmcnt(4)\n"
    "ds_read_b64_tr_b16 a[154:155], v239 offset:16640\n"
    "v_add_u32 v231, s60, v231\n"
    "v_mfma_f32_16x16x32_bf16 v[204:207], a[88:91], a[8:11], v[204:207]\n"
    "s_waitcnt lgkmcnt(3)\n"
    "buffer_store_dwordx4 a[244:247], v235, s[48:51], s74 offen nt\n"
    "v_mfma_f32_16x16x32_bf16 v[204:207], a[92:95], a[12:15], v[204:207]\n"
    "ds_read_b64_tr_b16 a[144:145], v238 offset:256\n"
    "v_cvt_pk_bf16_f32 v161, v194, v195\n"
    "v_exp_f32 v198, v198\n"
    "v_mfma_f32_16x16x32_bf16 v[192:195], a[64:67], a[16:19], v[212:215]\n"
    "ds_read_b64_tr_b16 a[190:191], v239 offset:16896\n"
    "v_exp_f32 v200, v200\n"
    "v_mfma_f32_16x16x32_bf16 v[192:195], a[68:71], a[20:23], v[192:195]\n"
    "ds_read_b64_tr_b16 a[152:153], v239 offset:256\n"
    "v_cvt_pk_bf16_f32 v163, v198, v199\n"
    "v_exp_f32 v201, v201\n"
    "v_mfma_f32_16x16x32_bf16 v[192:195], a[72:75], a[24:27], v[192:195]\n"
    "v_cvt_pk_bf16_f32 v162, v196, v197\n"
    "v_exp_f32 v207, v207\n"
    "v_mfma_f32_16x16x32_bf16 v[192:195], a[76:79], a[28:31], v[192:195]\n"
    "ds_read_b64_tr_b16 a[12:13], v239 offset:37376\n"
    "v_add_u32 v233, s60, v233\n"
    "v_mfma_f32_16x16x32_bf16 v[196:199], a[96:99], a[16:19], v[212:215]\n"
    "ds_read_b128 a[248:251], v234 offset:1024\n"
    "v_exp_f32 v203, v203\n"
    "v_exp_f32 v204, v204\n"
    "v_mfma_f32_16x16x32_bf16 v[196:199], a[100:103], a[20:23], v[196:199]\n"
    "v_exp_f32 v202, v202\n"
    "v_mfma_f32_16x16x32_bf16 v[196:199], a[104:107], a[24:27], v[196:199]\n"
    "v_cvt_pk_bf16_f32 v164, v200, v201\n"
    "v_exp_f32 v206, v206\n"
    "v_mfma_f32_16x16x32_bf16 v[196:199], a[108:111], a[28:31], v[196:199]\n"
    "ds_read_b64_tr_b16 a[204:205], v239 offset:768\n"
    "v_cvt_pk_bf16_f32 v165, v202, v203\n"
    "v_add_u32 v234, 2048, v234\n"
    "v_mfma_f32_16x16x32_bf16 v[200:203], a[112:115], a[16:19], v[212:215]\n"
    "ds_read_b64_tr_b16 a[2:3], v238 offset:20992\n"
    "v_exp_f32 v205, v205\n"
    "v_mfma_f32_16x16x32_bf16 v[200:203], a[116:119], a[20:23], v[200:203]\n"
    "ds_read_b64_tr_b16 a[4:5], v238 offset:37376\n"
    "v_cvt_pk_bf16_f32 v166, v204, v205\n"
    "v_mfma_f32_16x16x32_bf16 v[200:203], a[120:123], a[24:27], v[200:203]\n"
    "s_waitcnt lgkmcnt(3)\n"
    "buffer_store_dwordx4 a[248:251], v235, s[48:51], s74 offen offset:2048 nt\n"
    "v_exp_f32 v199, v199\n"
    "v_mfma_f32_16x16x32_bf16 v[200:203], a[124:127], a[28:31], v[200:203]\n"
    "s_mov_b32 s48, s76\n"
    "ds_read_b64_tr_b16 a[156:157], v239 offset:33024\n"
    "v_cvt_pk_bf16_f32 v167, v206, v207\n"
    "v_mfma_f32_16x16x32_bf16 v[204:207], a[80:83], a[16:19], v[212:215]\n"
    "s_mov_b32 s49, s77\n"
    "v_exp_f32 v194, v194\n"
    "v_mfma_f32_16x16x32_bf16 v[204:207], a[84:87], a[20:23], v[204:207]\n"
    "s_mov_b32 s74, s79\n"
    "ds_read_b64_tr_b16 a[158:159], v239 offset:49408\n"
    "v_exp_f32 v195, v195\n"
    "v_mfma_f32_16x16x32_bf16 v[204:207], a[88:91], a[24:27], v[204:207]\n"
    "s_add_u32 s73, s79, 4096\n"
    "ds_read_b64_tr_b16 a[210:211], v239 offset:49920\n"
    "v_add_u32 v232, s60, v232\n"
    "v_mfma_f32_16x16x32_bf16 v[204:207], a[92:95], a[28:31], v[204:207]\n"
    "s_add_u32 s52, s79, 8192\n"
    "ds_read_b64_tr_b16 a[182:183], v238 offset:16896\n"
    "v_cvt_pk_bf16_f32 v169, v194, v195\n"
    "v_exp_f32 v196, v196\n"
    "v_add_u32 v234, s58, v234\n"
    "v_smfmac_f32_16x16x64_bf16 a[160:163], a[176:179], v[128:135], v237\n"
    "s_add_u32 s53, s79, 12288\n"
    "ds_read_b64_tr_b16 a[146:147], v238 offset:16640\n"
    "v_mfma_f32_16x16x32_bf16 v[0:3], a[128:131], v[128:131], v[0:3]\n"
    "ds_read_b64_tr_b16 a[18:19], v238 offset:21248\n"
    "v_exp_f32 v193, v193\n"
    "v_mfma_f32_16x16x32_bf16 v[0:3], a[132:135], v[132:135], v[0:3]\n"
    "ds_read_b64_tr_b16 a[192:193], v239 offset:33280\n"
    "v_exp_f32 v192, v192\n"
    "v_mfma_f32_16x16x32_bf16 v[0:3], a[212:215], v[160:163], v[0:3]\n"
    "ds_read_b64_tr_b16 a[186:187], v238 offset:49664\n"
    "v_exp_f32 v201, v201\n"
    "v_mfma_f32_16x16x32_bf16 v[0:3], a[216:219], v[164:167], v[0:3]\n"
    "ds_read_b64_tr_b16 a[196:197], v238 offset:768\n"
    "v_exp_f32 v197, v197\n"
    "v_smfmac_f32_16x16x64_bf16 a[160:163], a[176:179], v[160:167], v237\n"
    "ds_read_b64_tr_b16 a[208:209], v239 offset:33536\n"
    "v_mfma_f32_16x16x32_bf16 v[16:19], a[136:139], v[128:131], v[16:19]\n"
    "ds_read_b64_tr_b16 a[22:23], v238 offset:54016\n"
    "v_add_u32 v230, s60, v230\n"
    "v_mfma_f32_16x16x32_bf16 v[16:19], a[140:143], v[132:135], v[16:19]\n"
    "ds_read_b64_tr_b16 a[188:189], v239 offset:512\n"
    "v_cvt_pk_bf16_f32 v168, v192, v193\n"
    "v_mfma_f32_16x16x32_bf16 v[16:19], a[220:223], v[160:163], v[16:19]\n"
    "ds_read_b64_tr_b16 a[202:203], v238 offset:49920\n"
    "v_exp_f32 v200, v200\n"
    "v_mfma_f32_16x16x32_bf16 v[16:19], a[224:227], v[164:167], v[16:19]\n"
    "ds_read_b64_tr_b16 a[6:7], v238 offset:53760\n"
    "v_exp_f32 v203, v203\n"
    "v_mfma_f32_16x16x32_bf16 v[192:195], a[64:67], a[32:35], v[248:251]\n"
    "ds_read_b64_tr_b16 a[28:29], v239 offset:37632\n"
    "v_exp_f32 v198, v198\n"
    "v_mfma_f32_16x16x32_bf16 v[192:195], a[68:71], a[36:39], v[192:195]\n"
    "ds_read_b64_tr_b16 a[194:195], v239 offset:49664\n"
    "v_accvgpr_read_b32 v217, a160\n"
    "v_mfma_f32_16x16x32_bf16 v[192:195], a[72:75], a[40:43], v[192:195]\n"
    "ds_read_b64_tr_b16 a[20:21], v238 offset:37632\n"
    "v_accvgpr_read_b32 v216, a161\n"
    "v_mfma_f32_16x16x32_bf16 v[192:195], a[76:79], a[44:47], v[192:195]\n"
    "s_waitcnt lgkmcnt(12)\n"
    "ds_read_b64_tr_b16 a[26:27], v239 offset:21248\n"
    "v_add_f32 v217, v217, v216\n"
    "v_mfma_f32_16x16x32_bf16 v[32:35], a[144:147], v[128:131], v[32:35]\n"
    "v_rcp_f32 v216, v217\n"
    "v_mfma_f32_16x16x32_bf16 v[32:35], a[148:151], v[132:135], v[32:35]\n"
    "v_cvt_pk_bf16_f32 v171, v198, v199\n"
    "v_exp_f32 v202, v202\n"
    "v_mfma_f32_16x16x32_bf16 v[32:35], a[228:231], v[160:163], v[32:35]\n"
    "ds_read_b64_tr_b16 a[198:199], v238 offset:17152\n"
    "v_cvt_pk_bf16_f32 v172, v200, v201\n"
    "v_mfma_f32_16x16x32_bf16 v[32:35], a[232:235], v[164:167], v[32:35]\n"
    "ds_read_b64_tr_b16 a[0:1], v238 offset:4608\n"
    "v_mov_b32 v217, v216\n"
    "v_mfma_f32_16x16x32_bf16 v[48:51], a[152:155], v[128:131], v[48:51]\n"
    "ds_read_b64_tr_b16 a[10:11], v239 offset:20992\n"
    "v_cvt_pk_bf16_f32 v173, v202, v203\n"
    "v_mfma_f32_16x16x32_bf16 v[48:51], a[156:159], v[132:135], v[48:51]\n"
    "ds_read_b64_tr_b16 a[184:185], v238 offset:33280\n"
    "v_cvt_pk_bf16_f32 v170, v196, v197\n"
    "v_exp_f32 v194, v194\n"
    "v_mfma_f32_16x16x32_bf16 v[48:51], a[236:239], v[160:163], v[48:51]\n"
    "ds_read_b64_tr_b16 a[14:15], v239 offset:53760\n"
    "v_exp_f32 v204, v204\n"
    "v_cmp_nle_f32 vcc, s63, v216\n"
    "v_mul_f32 v3, v216, v3\n"
    "v_pk_mul_f32 v[16:17], v[16:17], v[216:217]\n"
    "v_pk_mul_f32 v[18:19], v[18:19], v[216:217]\n"
    "v_mfma_f32_16x16x32_bf16 v[48:51], a[240:243], v[164:167], v[48:51]\n"
    "s_or_b64 s[66:67], s[66:67], vcc\n"
    "ds_read_b64_tr_b16 a[8:9], v239 offset:4608\n"
    "v_mul_f32 v32, v216, v32\n"
    "v_mfma_f32_16x16x32_bf16 v[196:199], a[96:99], a[32:35], v[248:251]\n"
    "v_mul_f32 v1, v216, v1\n"
    "v_mul_f32 v35, v216, v35\n"
    "v_mfma_f32_16x16x32_bf16 v[196:199], a[100:103], a[36:39], v[196:199]\n"
    "v_mul_f32 v2, v216, v2\n"
    "v_mul_f32 v33, v216, v33\n"
    "v_mfma_f32_16x16x32_bf16 v[196:199], a[104:107], a[40:43], v[196:199]\n"
    "ds_read_b64_tr_b16 a[16:17], v238 offset:4864\n"
    "v_cvt_pk_bf16_f32 v16, v16, v17\n"
    "v_cvt_pk_bf16_f32 v17, v18, v19\n"
    "v_mul_f32 v34, v216, v34\n"
    "v_mfma_f32_16x16x32_bf16 v[196:199], a[108:111], a[44:47], v[196:199]\n"
    "v_cvt_pk_bf16_f32 v32, v32, v33\n"
    "v_cvt_pk_bf16_f32 v33, v34, v35\n"
    "v_mfma_f32_16x16x32_bf16 v[64:67], a[180:183], v[128:131], v[64:67]\n"
    "s_waitcnt lgkmcnt(3)\n"
    "ds_write_b64 v232, v[32:33] offset:0\n"
    "v_exp_f32 v207, v207\n"
    "v_pk_mul_f32 v[48:49], v[48:49], v[216:217]\n"
    "v_mfma_f32_16x16x32_bf16 v[64:67], a[184:187], v[132:135], v[64:67]\n"
    "v_mul_f32 v0, v216, v0\n"
    "v_cvt_pk_bf16_f32 v0, v0, v1\n"
    "v_cvt_pk_bf16_f32 v48, v48, v49\n"
    "v_mfma_f32_16x16x32_bf16 v[64:67], a[0:3], v[160:163], v[64:67]\n"
    "ds_write_b64 v231, v[16:17] offset:0\n"
    "v_exp_f32 v193, v193\n"
    "v_pk_mul_f32 v[50:51], v[50:51], v[216:217]\n"
    "v_mfma_f32_16x16x32_bf16 v[64:67], a[4:7], v[164:167], v[64:67]\n"
    "v_cvt_pk_bf16_f32 v1, v2, v3\n"
    "v_cvt_pk_bf16_f32 v49, v50, v51\n"
    "v_mfma_f32_16x16x32_bf16 v[80:83], a[188:191], v[128:131], v[80:83]\n"
    "ds_write_b64 v233, v[48:49] offset:0\n"
    "v_mfma_f32_16x16x32_bf16 v[80:83], a[192:195], v[132:135], v[80:83]\n"
    "s_waitcnt lgkmcnt(4)\n"
    "v_exp_f32 v192, v192\n"
    "v_mfma_f32_16x16x32_bf16 v[80:83], a[8:11], v[160:163], v[80:83]\n"
    "ds_write_b64 v230, v[0:1] offset:0\n"
    "v_mfma_f32_16x16x32_bf16 v[80:83], a[12:15], v[164:167], v[80:83]\n"
    "v_exp_f32 v206, v206\n"
    "v_mfma_f32_16x16x32_bf16 v[200:203], a[112:115], a[32:35], v[248:251]\n"
    "ds_read_b64_tr_b16 a[24:25], v239 offset:4864\n"
    "v_mul_f32 v65, v216, v65\n"
    "v_mfma_f32_16x16x32_bf16 v[200:203], a[116:119], a[36:39], v[200:203]\n"
    "ds_read_b128 a[244:247], v234 offset:0\n"
    "v_mul_f32 v64, v216, v64\n"
    "v_mul_f32 v66, v216, v66\n"
    "v_mfma_f32_16x16x32_bf16 v[200:203], a[120:123], a[40:43], v[200:203]\n"
    "ds_read_b128 a[248:251], v234 offset:1024\n"
    "v_cvt_pk_bf16_f32 v64, v64, v65\n"
    "v_mfma_f32_16x16x32_bf16 v[200:203], a[124:127], a[44:47], v[200:203]\n"
    "ds_read_b64_tr_b16 a[30:31], v239 offset:54016\n"
    "v_mul_f32 v67, v216, v67\n"
    "v_mfma_f32_16x16x32_bf16 v[96:99], a[196:199], v[128:131], v[96:99]\n"
    "v_exp_f32 v205, v205\n"
    "v_cvt_pk_bf16_f32 v65, v66, v67\n"
    "v_mfma_f32_16x16x32_bf16 v[96:99], a[200:203], v[132:135], v[96:99]\n"
    "s_waitcnt lgkmcnt(2)\n"
    "buffer_store_dwordx4 a[244:247], v235, s[48:51], s74 offen offset:0 nt\n"
    "v_pk_mul_f32 v[82:83], v[82:83], v[216:217]\n"
    "v_mfma_f32_16x16x32_bf16 v[96:99], a[16:19], v[160:163], v[96:99]\n"
    "s_waitcnt lgkmcnt(1)\n"
    "buffer_store_dwordx4 a[248:251], v235, s[48:51], s74 offen offset:2048 nt\n"
    "v_cvt_pk_bf16_f32 v176, v192, v193\n"
    "v_pk_mul_f32 v[80:81], v[80:81], v[216:217]\n"
    "v_mfma_f32_16x16x32_bf16 v[96:99], a[20:23], v[164:167], v[96:99]\n"
    "ds_write_b64 v230, v[64:65] offset:8192\n"
    "v_mfma_f32_16x16x32_bf16 v[112:115], a[204:207], v[128:131], v[112:115]\n"
    "v_exp_f32 v197, v197\n"
    "v_mfma_f32_16x16x32_bf16 v[112:115], a[208:211], v[132:135], v[112:115]\n"
    "v_cvt_pk_bf16_f32 v174, v204, v205\n"
    "v_cvt_pk_bf16_f32 v175, v206, v207\n"
    "v_cvt_pk_bf16_f32 v80, v80, v81\n"
    "v_mfma_f32_16x16x32_bf16 v[112:115], a[24:27], v[160:163], v[112:115]\n"
    "s_waitcnt lgkmcnt(1)\n"
    "v_exp_f32 v203, v203\n"
    "v_mfma_f32_16x16x32_bf16 v[112:115], a[28:31], v[164:167], v[112:115]\n"
    "v_exp_f32 v200, v200\n"
    "v_mul_f32 v96, v216, v96\n"
    "v_mfma_f32_16x16x32_bf16 v[204:207], a[80:83], a[32:35], v[248:251]\n"
    "v_cvt_pk_bf16_f32 v81, v82, v83\n"
    "v_mul_f32 v99, v216, v99\n"
    "v_mfma_f32_16x16x32_bf16 v[204:207], a[84:87], a[36:39], v[204:207]\n"
    "ds_write_b64 v231, v[80:81] offset:8192\n"
    "v_exp_f32 v201, v201\n"
    "v_mfma_f32_16x16x32_bf16 v[204:207], a[88:91], a[40:43], v[204:207]\n"
    "v_mul_f32 v97, v216, v97\n"
    "v_cvt_pk_bf16_f32 v96, v96, v97\n"
    "v_mfma_f32_16x16x32_bf16 v[204:207], a[92:95], a[44:47], v[204:207]\n"
    "v_exp_f32 v198, v198\n"
    "v_smfmac_f32_16x16x64_bf16 a[164:167], a[176:179], v[136:143], v237\n"
    "v_exp_f32 v196, v196\n"
    "v_mfma_f32_16x16x32_bf16 v[4:7], a[128:131], v[136:139], v[4:7]\n"
    "v_mul_f32 v98, v216, v98\n"
    "v_cvt_pk_bf16_f32 v97, v98, v99\n"
    "v_mfma_f32_16x16x32_bf16 v[4:7], a[132:135], v[140:143], v[4:7]\n"
    "ds_write_b64 v232, v[96:97] offset:8192\n"
    "v_exp_f32 v205, v205\n"
    "v_pk_mul_f32 v[112:113], v[112:113], v[216:217]\n"
    "v_pk_mul_f32 v[114:115], v[114:115], v[216:217]\n"
    "v_mfma_f32_16x16x32_bf16 v[4:7], a[212:215], v[168:171], v[4:7]\n"
    "v_exp_f32 v202, v202\n"
    "v_mfma_f32_16x16x32_bf16 v[4:7], a[216:219], v[172:175], v[4:7]\n"
    "v_cvt_pk_bf16_f32 v178, v196, v197\n"
    "v_cvt_pk_bf16_f32 v112, v112, v113\n"
    "v_smfmac_f32_16x16x64_bf16 a[164:167], a[176:179], v[168:175], v237\n"
    "v_cvt_pk_bf16_f32 v113, v114, v115\n"
    "v_mfma_f32_16x16x32_bf16 v[20:23], a[136:139], v[136:139], v[20:23]\n"
    "ds_write_b64 v233, v[112:113] offset:8192\n"
    "v_mfma_f32_16x16x32_bf16 v[20:23], a[140:143], v[140:143], v[20:23]\n"
    "v_exp_f32 v195, v195\n"
    "v_mfma_f32_16x16x32_bf16 v[20:23], a[220:223], v[168:171], v[20:23]\n"
    "ds_read_b128 a[252:255], v234 offset:8192\n"
    "v_exp_f32 v199, v199\n"
    "v_mfma_f32_16x16x32_bf16 v[20:23], a[224:227], v[172:175], v[20:23]\n"
    "ds_read_b128 a[244:247], v234 offset:9216\n"
    "v_cvt_pk_bf16_f32 v177, v194, v195\n"
    "v_mfma_f32_16x16x32_bf16 v[192:195], a[64:67], a[48:51], v[252:255]\n"
    "v_exp_f32 v204, v204\n"
    "v_mfma_f32_16x16x32_bf16 v[192:195], a[68:71], a[52:55], v[192:195]\n"
    "v_cvt_pk_bf16_f32 v181, v202, v203\n"
    "v_accvgpr_read_b32 v219, a164\n"
    "v_mfma_f32_16x16x32_bf16 v[192:195], a[72:75], a[56:59], v[192:195]\n"
    "s_waitcnt lgkmcnt(1)\n"
    "buffer_store_dwordx4 a[252:255], v235, s[48:51], s74 offen offset:128 nt\n"
    "v_mfma_f32_16x16x32_bf16 v[192:195], a[76:79], a[60:63], v[192:195]\n"
    "v_accvgpr_read_b32 v218, a165\n"
    "v_add_f32 v219, v219, v218\n"
    "v_mfma_f32_16x16x32_bf16 v[36:39], a[144:147], v[136:139], v[36:39]\n"
    "v_rcp_f32 v218, v219\n"
    "v_mfma_f32_16x16x32_bf16 v[36:39], a[148:151], v[140:143], v[36:39]\n"
    "s_waitcnt lgkmcnt(0)\n"
    "buffer_store_dwordx4 a[244:247], v235, s[48:51], s74 offen offset:2176 nt\n"
    "v_mfma_f32_16x16x32_bf16 v[36:39], a[228:231], v[168:171], v[36:39]\n"
    "v_exp_f32 v207, v207\n"
    "v_mfma_f32_16x16x32_bf16 v[36:39], a[232:235], v[172:175], v[36:39]\n"
    "v_exp_f32 v192, v192\n"
    "v_mfma_f32_16x16x32_bf16 v[52:55], a[152:155], v[136:139], v[52:55]\n"
    "v_cvt_pk_bf16_f32 v179, v198, v199\n"
    "v_mov_b32 v219, v218\n"
    "v_mfma_f32_16x16x32_bf16 v[52:55], a[156:159], v[140:143], v[52:55]\n"
    "v_mul_f32 v4, v218, v4\n"
    "v_mul_f32 v7, v218, v7\n"
    "v_mfma_f32_16x16x32_bf16 v[52:55], a[236:239], v[168:171], v[52:55]\n"
    "v_cmp_nle_f32 vcc, s63, v218\n"
    "v_mul_f32 v5, v218, v5\n"
    "v_mfma_f32_16x16x32_bf16 v[52:55], a[240:243], v[172:175], v[52:55]\n"
    "s_or_b64 s[66:67], s[66:67], vcc\n"
    "v_mul_f32 v36, v218, v36\n"
    "v_mul_f32 v37, v218, v37\n"
    "v_mfma_f32_16x16x32_bf16 v[196:199], a[96:99], a[48:51], v[252:255]\n"
    "v_cvt_pk_bf16_f32 v180, v200, v201\n"
    "v_mul_f32 v38, v218, v38\n"
    "v_mfma_f32_16x16x32_bf16 v[196:199], a[100:103], a[52:55], v[196:199]\n"
    "v_mul_f32 v6, v218, v6\n"
    "v_cvt_pk_bf16_f32 v4, v4, v5\n"
    "v_mfma_f32_16x16x32_bf16 v[196:199], a[104:107], a[56:59], v[196:199]\n"
    "v_cvt_pk_bf16_f32 v5, v6, v7\n"
    "v_mul_f32 v39, v218, v39\n"
    "v_cvt_pk_bf16_f32 v36, v36, v37\n"
    "v_mfma_f32_16x16x32_bf16 v[196:199], a[108:111], a[60:63], v[196:199]\n"
    "ds_write_b64 v230, v[4:5] offset:2048\n"
    "v_exp_f32 v193, v193\n"
    "v_pk_mul_f32 v[20:21], v[20:21], v[218:219]\n"
    "v_pk_mul_f32 v[22:23], v[22:23], v[218:219]\n"
    "v_pk_mul_f32 v[52:53], v[52:53], v[218:219]\n"
    "v_pk_mul_f32 v[54:55], v[54:55], v[218:219]\n"
    "v_mfma_f32_16x16x32_bf16 v[68:71], a[180:183], v[136:139], v[68:71]\n"
    "v_cvt_pk_bf16_f32 v37, v38, v39\n"
    "v_cvt_pk_bf16_f32 v52, v52, v53\n"
    "v_mfma_f32_16x16x32_bf16 v[68:71], a[184:187], v[140:143], v[68:71]\n"
    "ds_write_b64 v232, v[36:37] offset:2048\n"
    "v_mfma_f32_16x16x32_bf16 v[68:71], a[0:3], v[168:171], v[68:71]\n"
    "v_exp_f32 v195, v195\n"
    "v_mfma_f32_16x16x32_bf16 v[68:71], a[4:7], v[172:175], v[68:71]\n"
    "v_cvt_pk_bf16_f32 v20, v20, v21\n"
    "v_cvt_pk_bf16_f32 v53, v54, v55\n"
    "v_mfma_f32_16x16x32_bf16 v[84:87], a[188:191], v[136:139], v[84:87]\n"
    "ds_write_b64 v233, v[52:53] offset:2048\n"
    "v_mfma_f32_16x16x32_bf16 v[84:87], a[192:195], v[140:143], v[84:87]\n"
    "v_cvt_pk_bf16_f32 v182, v204, v205\n"
    "v_cvt_pk_bf16_f32 v21, v22, v23\n"
    "v_mfma_f32_16x16x32_bf16 v[84:87], a[8:11], v[168:171], v[84:87]\n"
    "ds_write_b64 v231, v[20:21] offset:2048\n"
    "v_mfma_f32_16x16x32_bf16 v[84:87], a[12:15], v[172:175], v[84:87]\n"
    "v_exp_f32 v206, v206\n"
    "v_mfma_f32_16x16x32_bf16 v[200:203], a[112:115], a[48:51], v[252:255]\n"
    "v_mul_f32 v70, v218, v70\n"
    "v_mul_f32 v71, v218, v71\n"
    "v_mfma_f32_16x16x32_bf16 v[200:203], a[116:119], a[52:55], v[200:203]\n"
    "ds_read_b128 a[32:35], v234 offset:2048\n"
    "v_mul_f32 v68, v218, v68\n"
    "v_mfma_f32_16x16x32_bf16 v[200:203], a[120:123], a[56:59], v[200:203]\n"
    "ds_read_b128 a[36:39], v234 offset:3072\n"
    "v_mul_f32 v69, v218, v69\n"
    "v_mfma_f32_16x16x32_bf16 v[200:203], a[124:127], a[60:63], v[200:203]\n"
    "v_exp_f32 v196, v196\n"
    "v_mfma_f32_16x16x32_bf16 v[100:103], a[196:199], v[136:139], v[100:103]\n"
    "v_exp_f32 v198, v198\n"
    "v_mfma_f32_16x16x32_bf16 v[100:103], a[200:203], v[140:143], v[100:103]\n"
    "s_waitcnt lgkmcnt(1)\n"
    "buffer_store_dwordx4 a[32:35], v235, s[48:51], s73 offen offset:0 nt\n"
    "v_cvt_pk_bf16_f32 v183, v206, v207\n"
    "v_pk_mul_f32 v[86:87], v[86:87], v[218:219]\n"
    "v_mfma_f32_16x16x32_bf16 v[100:103], a[16:19], v[168:171], v[100:103]\n"
    "v_exp_f32 v202, v202\n"
    "v_mfma_f32_16x16x32_bf16 v[100:103], a[20:23], v[172:175], v[100:103]\n"
    "s_waitcnt lgkmcnt(0)\n"
    "buffer_store_dwordx4 a[36:39], v235, s[48:51], s73 offen offset:2048 nt\n"
    "v_cvt_pk_bf16_f32 v68, v68, v69\n"
    "v_pk_mul_f32 v[84:85], v[84:85], v[218:219]\n"
    "v_mfma_f32_16x16x32_bf16 v[116:119], a[204:207], v[136:139], v[116:119]\n"
    "v_cvt_pk_bf16_f32 v69, v70, v71\n"
    "v_cvt_pk_bf16_f32 v84, v84, v85\n"
    "v_mfma_f32_16x16x32_bf16 v[116:119], a[208:211], v[140:143], v[116:119]\n"
    "ds_write_b64 v230, v[68:69] offset:10240\n"
    "v_mfma_f32_16x16x32_bf16 v[116:119], a[24:27], v[168:171], v[116:119]\n"
    "v_exp_f32 v199, v199\n"
    "v_mfma_f32_16x16x32_bf16 v[116:119], a[28:31], v[172:175], v[116:119]\n"
    "v_exp_f32 v201, v201\n"
    "v_mfma_f32_16x16x32_bf16 v[204:207], a[80:83], a[48:51], v[252:255]\n"
    "v_cvt_pk_bf16_f32 v85, v86, v87\n"
    "v_mul_f32 v103, v218, v103\n"
    "v_mfma_f32_16x16x32_bf16 v[204:207], a[84:87], a[52:55], v[204:207]\n"
    "ds_write_b64 v231, v[84:85] offset:10240\n"
    "v_mul_f32 v101, v218, v101\n"
    "v_mfma_f32_16x16x32_bf16 v[204:207], a[88:91], a[56:59], v[204:207]\n"
    "v_exp_f32 v200, v200\n"
    "v_mfma_f32_16x16x32_bf16 v[204:207], a[92:95], a[60:63], v[204:207]\n"
    "v_exp_f32 v197, v197\n"
    "v_smfmac_f32_16x16x64_bf16 a[168:171], a[176:179], v[144:151], v237\n"
    "v_mul_f32 v100, v218, v100\n"
    "v_mul_f32 v102, v218, v102\n"
    "v_cvt_pk_bf16_f32 v100, v100, v101\n"
    "v_cvt_pk_bf16_f32 v101, v102, v103\n"
    "v_pk_mul_f32 v[116:117], v[116:117], v[218:219]\n"
    "v_pk_mul_f32 v[118:119], v[118:119], v[218:219]\n"
    "v_mfma_f32_16x16x32_bf16 v[8:11], a[128:131], v[144:147], v[8:11]\n"
    "v_exp_f32 v203, v203\n"
    "v_mfma_f32_16x16x32_bf16 v[8:11], a[132:135], v[148:151], v[8:11]\n"
    "ds_write_b64 v232, v[100:101] offset:10240\n"
    "v_mfma_f32_16x16x32_bf16 v[8:11], a[212:215], v[176:179], v[8:11]\n"
    "v_exp_f32 v194, v194\n"
    "v_mfma_f32_16x16x32_bf16 v[8:11], a[216:219], v[180:183], v[8:11]\n"
    "v_cvt_pk_bf16_f32 v189, v202, v203\n"
    "v_cvt_pk_bf16_f32 v116, v116, v117\n"
    "v_smfmac_f32_16x16x64_bf16 a[168:171], a[176:179], v[176:183], v237\n"
    "v_cvt_pk_bf16_f32 v117, v118, v119\n"
    "v_mfma_f32_16x16x32_bf16 v[24:27], a[136:139], v[144:147], v[24:27]\n"
    "v_exp_f32 v207, v207\n"
    "v_mfma_f32_16x16x32_bf16 v[24:27], a[140:143], v[148:151], v[24:27]\n"
    "ds_write_b64 v233, v[116:117] offset:10240\n"
    "v_mfma_f32_16x16x32_bf16 v[24:27], a[220:223], v[176:179], v[24:27]\n"
    "v_exp_f32 v204, v204\n"
    "v_mfma_f32_16x16x32_bf16 v[24:27], a[224:227], v[180:183], v[24:27]\n"
    "ds_read_b128 a[40:43], v234 offset:10240\n"
    "v_cvt_pk_bf16_f32 v188, v200, v201\n"
    "v_mfma_f32_16x16x32_bf16 v[40:43], a[144:147], v[144:147], v[40:43]\n"
    "ds_read_b128 a[44:47], v234 offset:11264\n"
    "v_cvt_pk_bf16_f32 v184, v192, v193\n"
    "v_mfma_f32_16x16x32_bf16 v[40:43], a[148:151], v[148:151], v[40:43]\n"
    "v_accvgpr_read_b32 v221, a168\n"
    "v_accvgpr_read_b32 v220, a169\n"
    "v_mfma_f32_16x16x32_bf16 v[40:43], a[228:231], v[176:179], v[40:43]\n"
    "v_add_f32 v221, v221, v220\n"
    "v_rcp_f32 v220, v221\n"
    "v_mfma_f32_16x16x32_bf16 v[40:43], a[232:235], v[180:183], v[40:43]\n"
    "s_waitcnt lgkmcnt(1)\n"
    "buffer_store_dwordx4 a[40:43], v235, s[48:51], s73 offen offset:128 nt\n"
    "v_mfma_f32_16x16x32_bf16 v[56:59], a[152:155], v[144:147], v[56:59]\n"
    "v_cvt_pk_bf16_f32 v186, v196, v197\n"
    "v_mov_b32 v221, v220\n"
    "v_mfma_f32_16x16x32_bf16 v[56:59], a[156:159], v[148:151], v[56:59]\n"
    "s_waitcnt lgkmcnt(0)\n"
    "buffer_store_dwordx4 a[44:47], v235, s[48:51], s73 offen offset:2176 nt\n"
    "v_mul_f32 v9, v220, v9\n"
    "v_pk_mul_f32 v[24:25], v[24:25], v[220:221]\n"
    "v_pk_mul_f32 v[26:27], v[26:27], v[220:221]\n"
    "v_mfma_f32_16x16x32_bf16 v[56:59], a[236:239], v[176:179], v[56:59]\n"
    "v_mul_f32 v8, v220, v8\n"
    "v_mul_f32 v10, v220, v10\n"
    "v_mfma_f32_16x16x32_bf16 v[56:59], a[240:243], v[180:183], v[56:59]\n"
    "v_mul_f32 v11, v220, v11\n"
    "v_cvt_pk_bf16_f32 v8, v8, v9\n"
    "v_mfma_f32_16x16x32_bf16 v[72:75], a[180:183], v[144:147], v[72:75]\n"
    "v_mul_f32 v40, v220, v40\n"
    "v_mul_f32 v41, v220, v41\n"
    "v_mfma_f32_16x16x32_bf16 v[72:75], a[184:187], v[148:151], v[72:75]\n"
    "v_exp_f32 v206, v206\n"
    "v_cvt_pk_bf16_f32 v24, v24, v25\n"
    "v_mfma_f32_16x16x32_bf16 v[72:75], a[0:3], v[176:179], v[72:75]\n"
    "v_cmp_nle_f32 vcc, s63, v220\n"
    "v_cvt_pk_bf16_f32 v9, v10, v11\n"
    "v_mfma_f32_16x16x32_bf16 v[72:75], a[4:7], v[180:183], v[72:75]\n"
    "s_or_b64 s[66:67], s[66:67], vcc\n"
    "ds_write_b64 v230, v[8:9] offset:4096\n"
    "v_exp_f32 v205, v205\n"
    "v_pk_mul_f32 v[56:57], v[56:57], v[220:221]\n"
    "v_pk_mul_f32 v[58:59], v[58:59], v[220:221]\n"
    "v_mfma_f32_16x16x32_bf16 v[88:91], a[188:191], v[144:147], v[88:91]\n"
    "v_cvt_pk_bf16_f32 v185, v194, v195\n"
    "v_cvt_pk_bf16_f32 v56, v56, v57\n"
    "v_mfma_f32_16x16x32_bf16 v[88:91], a[192:195], v[148:151], v[88:91]\n"
    "v_cvt_pk_bf16_f32 v25, v26, v27\n"
    "v_cvt_pk_bf16_f32 v40, v40, v41\n"
    "v_mfma_f32_16x16x32_bf16 v[88:91], a[8:11], v[176:179], v[88:91]\n"
    "v_mul_f32 v42, v220, v42\n"
    "v_mul_f32 v43, v220, v43\n"
    "v_cvt_pk_bf16_f32 v57, v58, v59\n"
    "v_mfma_f32_16x16x32_bf16 v[88:91], a[12:15], v[180:183], v[88:91]\n"
    "ds_write_b64 v233, v[56:57] offset:4096\n"
    "v_mfma_f32_16x16x32_bf16 v[104:107], a[196:199], v[144:147], v[104:107]\n"
    "v_cvt_pk_bf16_f32 v41, v42, v43\n"
    "v_mul_f32 v75, v220, v75\n"
    "v_mfma_f32_16x16x32_bf16 v[104:107], a[200:203], v[148:151], v[104:107]\n"
    "ds_write_b64 v231, v[24:25] offset:4096\n"
    "v_mfma_f32_16x16x32_bf16 v[104:107], a[16:19], v[176:179], v[104:107]\n"
    "v_mul_f32 v72, v220, v72\n"
    "v_mul_f32 v74, v220, v74\n"
    "v_mfma_f32_16x16x32_bf16 v[104:107], a[20:23], v[180:183], v[104:107]\n"
    "ds_write_b64 v232, v[40:41] offset:4096\n"
    "v_mfma_f32_16x16x32_bf16 v[120:123], a[204:207], v[144:147], v[120:123]\n"
    "v_mul_f32 v73, v220, v73\n"
    "v_cvt_pk_bf16_f32 v72, v72, v73\n"
    "v_mfma_f32_16x16x32_bf16 v[120:123], a[208:211], v[148:151], v[120:123]\n"
    "ds_read_b128 a[52:55], v234 offset:5120\n"
    "v_cvt_pk_bf16_f32 v187, v198, v199\n"
    "v_mfma_f32_16x16x32_bf16 v[120:123], a[24:27], v[176:179], v[120:123]\n"
    "ds_read_b128 a[48:51], v234 offset:4096\n"
    "v_cvt_pk_bf16_f32 v190, v204, v205\n"
    "v_mfma_f32_16x16x32_bf16 v[120:123], a[28:31], v[180:183], v[120:123]\n"
    "v_cvt_pk_bf16_f32 v73, v74, v75\n"
    "v_mul_f32 v104, v220, v104\n"
    "v_smfmac_f32_16x16x64_bf16 a[172:175], a[176:179], v[152:159], v237\n"
    "v_cvt_pk_bf16_f32 v191, v206, v207\n"
    "v_mfma_f32_16x16x32_bf16 v[12:15], a[128:131], v[152:155], v[12:15]\n"
    "ds_write_b64 v230, v[72:73] offset:12288\n"
    "v_mfma_f32_16x16x32_bf16 v[12:15], a[132:135], v[156:159], v[12:15]\n"
    "s_waitcnt lgkmcnt(1)\n"
    "buffer_store_dwordx4 a[48:51], v235, s[48:51], s52 offen offset:0 nt\n"
    "v_mfma_f32_16x16x32_bf16 v[12:15], a[212:215], v[184:187], v[12:15]\n"
    "buffer_store_dwordx4 a[52:55], v235, s[48:51], s52 offen offset:2048 nt\n"
    "v_pk_mul_f32 v[88:89], v[88:89], v[220:221]\n"
    "v_pk_mul_f32 v[90:91], v[90:91], v[220:221]\n"
    "v_pk_mul_f32 v[122:123], v[122:123], v[220:221]\n"
    "v_mfma_f32_16x16x32_bf16 v[12:15], a[216:219], v[188:191], v[12:15]\n"
    "v_cvt_pk_bf16_f32 v88, v88, v89\n"
    "v_cvt_pk_bf16_f32 v89, v90, v91\n"
    "v_smfmac_f32_16x16x64_bf16 a[172:175], a[176:179], v[184:191], v237\n"
    "ds_write_b64 v231, v[88:89] offset:12288\n"
    "v_mul_f32 v107, v220, v107\n"
    "v_pk_mul_f32 v[120:121], v[120:121], v[220:221]\n"
    "v_mfma_f32_16x16x32_bf16 v[28:31], a[136:139], v[152:155], v[28:31]\n"
    "v_mul_f32 v105, v220, v105\n"
    "v_cvt_pk_bf16_f32 v104, v104, v105\n"
    "v_mfma_f32_16x16x32_bf16 v[28:31], a[140:143], v[156:159], v[28:31]\n"
    "v_mul_f32 v106, v220, v106\n"
    "v_cvt_pk_bf16_f32 v105, v106, v107\n"
    "v_mfma_f32_16x16x32_bf16 v[28:31], a[220:223], v[184:187], v[28:31]\n"
    "ds_write_b64 v232, v[104:105] offset:12288\n"
    "v_mfma_f32_16x16x32_bf16 v[28:31], a[224:227], v[188:191], v[28:31]\n"
    "v_cvt_pk_bf16_f32 v120, v120, v121\n"
    "v_cvt_pk_bf16_f32 v121, v122, v123\n"
    "v_mfma_f32_16x16x32_bf16 v[44:47], a[144:147], v[152:155], v[44:47]\n"
    "ds_write_b64 v233, v[120:121] offset:12288\n"
    "v_mfma_f32_16x16x32_bf16 v[44:47], a[148:151], v[156:159], v[44:47]\n"
    "v_accvgpr_read_b32 v223, a172\n"
    "v_accvgpr_read_b32 v222, a173\n"
    "v_mfma_f32_16x16x32_bf16 v[44:47], a[228:231], v[184:187], v[44:47]\n"
    "ds_read_b128 a[60:63], v234 offset:13312\n"
    "v_mfma_f32_16x16x32_bf16 v[44:47], a[232:235], v[188:191], v[44:47]\n"
    "ds_read_b128 a[56:59], v234 offset:12288\n"
    "v_add_f32 v223, v223, v222\n"
    "v_mfma_f32_16x16x32_bf16 v[60:63], a[152:155], v[152:155], v[60:63]\n"
    "v_rcp_f32 v222, v223\n"
    "v_mfma_f32_16x16x32_bf16 v[60:63], a[156:159], v[156:159], v[60:63]\n"
    "v_mov_b32 v223, v222\n"
    "v_mfma_f32_16x16x32_bf16 v[60:63], a[236:239], v[184:187], v[60:63]\n"
    "s_waitcnt lgkmcnt(1)\n"
    "buffer_store_dwordx4 a[60:63], v235, s[48:51], s52 offen offset:2176 nt\n"
    "v_mfma_f32_16x16x32_bf16 v[60:63], a[240:243], v[188:191], v[60:63]\n"
    "s_waitcnt lgkmcnt(0)\n"
    "buffer_store_dwordx4 a[56:59], v235, s[48:51], s52 offen offset:128 nt\n"
    "v_pk_mul_f32 v[28:29], v[28:29], v[222:223]\n"
    "v_pk_mul_f32 v[30:31], v[30:31], v[222:223]\n"
    "v_mfma_f32_16x16x32_bf16 v[76:79], a[180:183], v[152:155], v[76:79]\n"
    "v_mul_f32 v15, v222, v15\n"
    "v_cvt_pk_bf16_f32 v28, v28, v29\n"
    "v_cvt_pk_bf16_f32 v29, v30, v31\n"
    "v_mfma_f32_16x16x32_bf16 v[76:79], a[184:187], v[156:159], v[76:79]\n"
    "v_mul_f32 v14, v222, v14\n"
    "v_mul_f32 v45, v222, v45\n"
    "v_mfma_f32_16x16x32_bf16 v[76:79], a[0:3], v[184:187], v[76:79]\n"
    "v_cmp_nle_f32 vcc, s63, v222\n"
    "v_mul_f32 v12, v222, v12\n"
    "v_mul_f32 v13, v222, v13\n"
    "v_mfma_f32_16x16x32_bf16 v[76:79], a[4:7], v[188:191], v[76:79]\n"
    "s_or_b64 s[66:67], s[66:67], vcc\n"
    "v_cvt_pk_bf16_f32 v12, v12, v13\n"
    "v_cvt_pk_bf16_f32 v13, v14, v15\n"
    "v_mfma_f32_16x16x32_bf16 v[92:95], a[188:191], v[152:155], v[92:95]\n"
    "s_cmp_lg_u64 s[66:67], 0\n"
    "ds_write_b64 v230, v[12:13] offset:6144\n"
    "v_mfma_f32_16x16x32_bf16 v[92:95], a[192:195], v[156:159], v[92:95]\n"
    "s_addc_u32 s68, s68, s68\n"
    "v_mul_f32 v44, v222, v44\n"
    "v_mul_f32 v47, v222, v47\n"
    "v_mfma_f32_16x16x32_bf16 v[92:95], a[8:11], v[184:187], v[92:95]\n"
    "ds_write_b64 v231, v[28:29] offset:6144\n"
    "v_mul_f32 v46, v222, v46\n"
    "v_cvt_pk_bf16_f32 v44, v44, v45\n"
    "v_pk_mul_f32 v[60:61], v[60:61], v[222:223]\n"
    "v_pk_mul_f32 v[62:63], v[62:63], v[222:223]\n"
    "v_mfma_f32_16x16x32_bf16 v[92:95], a[12:15], v[188:191], v[92:95]\n"
    "v_cvt_pk_bf16_f32 v45, v46, v47\n"
    "v_mul_f32 v76, v222, v76\n"
    "v_mfma_f32_16x16x32_bf16 v[108:111], a[196:199], v[152:155], v[108:111]\n"
    "ds_write_b64 v232, v[44:45] offset:6144\n"
    "v_mfma_f32_16x16x32_bf16 v[108:111], a[200:203], v[156:159], v[108:111]\n"
    "v_cvt_pk_bf16_f32 v60, v60, v61\n"
    "v_cvt_pk_bf16_f32 v61, v62, v63\n"
    "v_mfma_f32_16x16x32_bf16 v[108:111], a[16:19], v[184:187], v[108:111]\n"
    "ds_write_b64 v233, v[60:61] offset:6144\n"
    "v_mfma_f32_16x16x32_bf16 v[108:111], a[20:23], v[188:191], v[108:111]\n"
    "ds_read_b128 a[68:71], v234 offset:7168\n"
    "v_mul_f32 v78, v222, v78\n"
    "v_mfma_f32_16x16x32_bf16 v[124:127], a[204:207], v[152:155], v[124:127]\n"
    "v_mul_f32 v77, v222, v77\n"
    "v_mul_f32 v79, v222, v79\n"
    "v_mfma_f32_16x16x32_bf16 v[124:127], a[208:211], v[156:159], v[124:127]\n"
    "v_cvt_pk_bf16_f32 v76, v76, v77\n"
    "v_cvt_pk_bf16_f32 v77, v78, v79\n"
    "v_mfma_f32_16x16x32_bf16 v[124:127], a[24:27], v[184:187], v[124:127]\n"
    "ds_read_b128 a[64:67], v234 offset:6144\n"
    "v_mfma_f32_16x16x32_bf16 v[124:127], a[28:31], v[188:191], v[124:127]\n"
    "s_waitcnt lgkmcnt(1)\n"
    "buffer_store_dwordx4 a[68:71], v235, s[48:51], s53 offen offset:2048 nt\n"
    "ds_write_b64 v230, v[76:77] offset:14336\n"
    "v_pk_mul_f32 v[92:93], v[92:93], v[222:223]\n"
    "v_pk_mul_f32 v[108:109], v[108:109], v[222:223]\n"
    "v_pk_mul_f32 v[94:95], v[94:95], v[222:223]\n"
    "s_waitcnt lgkmcnt(1)\n"
    "buffer_store_dwordx4 a[64:67], v235, s[48:51], s53 offen offset:0 nt\n"
    "v_cvt_pk_bf16_f32 v92, v92, v93\n"
    "v_cvt_pk_bf16_f32 v93, v94, v95\n"
    "v_pk_mul_f32 v[110:111], v[110:111], v[222:223]\n"
    "v_pk_mul_f32 v[124:125], v[124:125], v[222:223]\n"
    "v_pk_mul_f32 v[126:127], v[126:127], v[222:223]\n"
    "v_cvt_pk_bf16_f32 v124, v124, v125\n"
    "ds_write_b64 v231, v[92:93] offset:14336\n"
    "v_cvt_pk_bf16_f32 v108, v108, v109\n"
    "v_cvt_pk_bf16_f32 v109, v110, v111\n"
    "v_cvt_pk_bf16_f32 v125, v126, v127\n"
    "ds_write_b64 v232, v[108:109] offset:14336\n"
    "ds_write_b64 v233, v[124:125] offset:14336\n"
    "ds_read_b128 a[72:75], v234 offset:14336\n"
    "ds_read_b128 a[76:79], v234 offset:15360\n"
    "s_waitcnt lgkmcnt(1)\n"
    "buffer_store_dwordx4 a[72:75], v235, s[48:51], s53 offen offset:128 nt\n"
    "s_waitcnt lgkmcnt(0)\n"
    "buffer_store_dwordx4 a[76:79], v235, s[48:51], s53 offen offset:2176 nt\n"
    "s_branch LFIN%=\n"
    "LSPT0%=:\n"
    "ds_read_b32 v242, v241\n"
    "s_waitcnt lgkmcnt(0)\n"
    "v_cmp_gt_u32 vcc, v243, v242\n"
    "s_cbranch_vccnz LSPT0%=\n"
    "s_branch LSRT0%=\n"
    "LSPTo%=:\n"
    "ds_read_b32 v242, v241\n"
    "s_waitcnt lgkmcnt(0)\n"
    "v_cmp_gt_u32 vcc, v243, v242\n"
    "s_cbranch_vccnz LSPTo%=\n"
    "s_branch LSRTo%=\n"
    "LSPTe%=:\n"
    "ds_read_b32 v242, v241\n"
    "s_waitcnt lgkmcnt(0)\n"
    "v_cmp_gt_u32 vcc, v243, v242\n"
    "s_cbranch_vccnz LSPTe%=\n"
    "s_branch LSRTe%=\n"
    "LSPTo2%=:\n"
    "ds_read_b32 v242, v241\n"
    "s_waitcnt lgkmcnt(0)\n"
    "v_cmp_gt_u32 vcc, v243, v242\n"
    "s_cbranch_vccnz LSPTo2%=\n"
    "s_branch LSRTo2%=\n"
    "LSPP2%=:\n"
    "ds_read_b32 v242, v241\n"
    "s_waitcnt lgkmcnt(0)\n"
    "v_cmp_gt_u32 vcc, v243, v242\n"
    "s_cbranch_vccnz LSPP2%=\n"
    "s_branch LSRP2%=\n"
    "LFIN%=:\n"
    "LEND%=:\n"
    "s_mov_b32 %[fm], s68\n"
    : [fm] "=s"(fm)
    : [w] "s"(w), [ka] "s"(__builtin_amdgcn_kernarg_segment_ptr()), [blk] "s"(blockIdx.x)
    : "vcc","scc","m0","memory","s8","s9","s10","s11","s12","s13","s14","s15","s16","s17","s18","s19","s20","s21","s22","s23","s24","s25","s26","s27","s28","s29","s30","s31","s36","s37","s38","s39","s40","s41","s42","s43","s44","s45","s46","s47","s48","s49","s50","s51","s52","s53","s54","s55","s56","s57","s58","s59","s60","s61","s62","s63","s64","s65","s66","s67","s68","s69","s70","s71","s72","s73","s74","s75","s76","s77","s78","s79","s80","s81","s82","s83","s84","s85","s86","s87","s88","s89","s90","s91","s92","s93","s94","s95","s96","s97","s98","s99","v0","v1","v2","v3","v4","v5","v6","v7","v8","v9","v10","v11","v12","v13","v14","v15","v16","v17","v18","v19","v20","v21","v22","v23","v24","v25","v26","v27","v28","v29","v30","v31","v32","v33","v34","v35","v36","v37","v38","v39","v40","v41","v42","v43","v44","v45","v46","v47","v48","v49","v50","v51","v52","v53","v54","v55","v56","v57","v58","v59","v60","v61","v62","v63","v64","v65","v66","v67","v68","v69","v70","v71","v72","v73","v74","v75","v76","v77","v78","v79","v80","v81","v82","v83","v84","v85","v86","v87","v88","v89","v90","v91","v92","v93","v94","v95","v96","v97","v98","v99","v100","v101","v102","v103","v104","v105","v106","v107","v108","v109","v110","v111","v112","v113","v114","v115","v116","v117","v118","v119","v120","v121","v122","v123","v124","v125","v126","v127","v128","v129","v130","v131","v132","v133","v134","v135","v136","v137","v138","v139","v140","v141","v142","v143","v144","v145","v146","v147","v148","v149","v150","v151","v152","v153","v154","v155","v156","v157","v158","v159","v160","v161","v162","v163","v164","v165","v166","v167","v168","v169","v170","v171","v172","v173","v174","v175","v176","v177","v178","v179","v180","v181","v182","v183","v184","v185","v186","v187","v188","v189","v190","v191","v192","v193","v194","v195","v196","v197","v198","v199","v200","v201","v202","v203","v204","v205","v206","v207","v208","v209","v210","v211","v212","v213","v214","v215","v216","v217","v218","v219","v220","v221","v222","v223","v224","v225","v226","v227","v228","v229","v230","v231","v232","v233","v234","v235","v236","v237","v238","v239","v240","v241","v242","v243","v244","v245","v248","v249","v250","v251","v252","v253","v254","v255","a0","a1","a2","a3","a4","a5","a6","a7","a8","a9","a10","a11","a12","a13","a14","a15","a16","a17","a18","a19","a20","a21","a22","a23","a24","a25","a26","a27","a28","a29","a30","a31","a32","a33","a34","a35","a36","a37","a38","a39","a40","a41","a42","a43","a44","a45","a46","a47","a48","a49","a50","a51","a52","a53","a54","a55","a56","a57","a58","a59","a60","a61","a62","a63","a64","a65","a66","a67","a68","a69","a70","a71","a72","a73","a74","a75","a76","a77","a78","a79","a80","a81","a82","a83","a84","a85","a86","a87","a88","a89","a90","a91","a92","a93","a94","a95","a96","a97","a98","a99","a100","a101","a102","a103","a104","a105","a106","a107","a108","a109","a110","a111","a112","a113","a114","a115","a116","a117","a118","a119","a120","a121","a122","a123","a124","a125","a126","a127","a128","a129","a130","a131","a132","a133","a134","a135","a136","a137","a138","a139","a140","a141","a142","a143","a144","a145","a146","a147","a148","a149","a150","a151","a152","a153","a154","a155","a156","a157","a158","a159","a160","a161","a162","a163","a164","a165","a166","a167","a168","a169","a170","a171","a172","a173","a174","a175","a176","a177","a178","a179","a180","a181","a182","a183","a184","a185","a186","a187","a188","a189","a190","a191","a192","a193","a194","a195","a196","a197","a198","a199","a200","a201","a202","a203","a204","a205","a206","a207","a208","a209","a210","a211","a212","a213","a214","a215","a216","a217","a218","a219","a220","a221","a222","a223","a224","a225","a226","a227","a228","a229","a230","a231","a232","a233","a234","a235","a236","a237","a238","a239","a240","a241","a242","a243","a244","a245","a246","a247","a248","a249","a250","a251","a252","a253","a254","a255");
  unsigned lane;
  asm volatile("v_mbcnt_lo_u32_b32 %0, -1, 0\n\tv_mbcnt_hi_u32_b32 %0, -1, %0" : "=v"(lane));
  if (fm) {
    asm volatile("s_waitcnt vmcnt(0)" ::: "memory");
#pragma unroll 1
    for (int r = 0; r < 3; ++r) {
      if (!((fm >> (2 - r)) & 1u)) continue;
      const unsigned it = (blockIdx.x >> 3) + 32u * r;
      slow_rows(q, k, v, o, (int)(32u * (blockIdx.x & 7u) + it / 3u), (int)((it % 3u) * 256u + w * 64u + lane));
    }
  }
}

extern "C" void kernel_launch(void* const* d_in, const int* in_sizes, int n_in, void* d_out, int out_size, void* d_ws, size_t ws_size, hipStream_t stream) {
  (void)in_sizes; (void)n_in; (void)out_size; (void)ws_size;
  (void)hipFuncSetAttribute((const void*)mha_fwd, hipFuncAttributeMaxDynamicSharedMemorySize, 152576);
  (void)d_ws;
  hipLaunchKernelGGL(mha_fwd, dim3(256), dim3(256), 152576, stream, (const unsigned*)d_in[0], (const unsigned*)d_in[1], (const unsigned*)d_in[2], (unsigned*)d_out);
}
```
